```python
import torch

def _softmax(x, axis=-1):
    return torch.softmax(x, dim=axis)
def _gelu(x):
    return torch.nn.functional.gelu(x, approximate="tanh")
def _silu(x):
    return torch.nn.functional.silu(x)
def _sigmoid(x):
    return torch.sigmoid(x)
def _layernorm(x, axis=-1, eps=1e-5):
    m = x.mean(dim=axis, keepdim=True)
    v = x.var(dim=axis, keepdim=True, unbiased=False)
    return (x - m) / torch.sqrt(v + eps)
def _scatter_add(tgt, idx, src, axis=0):
    return tgt.clone().index_add_(axis, idx, src)
def _segment_sum(src, seg, n):
    out = torch.zeros((n,) + src.shape[1:], dtype=src.dtype)
    return out.index_add_(0, seg, src)
def _pad(x, pw):
    flat = tuple(v for pair in reversed(pw) for v in pair)
    return torch.nn.functional.pad(x, flat)

kernel_name = 'recurrent_scan__ssm_conv_h3__glu_gated_geglu__prompt'

def setup_inputs():

    _w = (lambda a: (lambda t: t.to(torch.bfloat16) if t.is_floating_point() else t)(torch.as_tensor(a)))
    def _A_setup():
        B, S, D = 1, 2048, 512
        ks = 4
        return {
            "q": _w((lambda *s: torch.randn(*s, dtype=torch.bfloat16) * 0.05)(B, S, D)),
            "k": _w((lambda *s: torch.randn(*s, dtype=torch.bfloat16) * 0.8)(B, S, D)),
            "v": _w((lambda *s: torch.randn(*s, dtype=torch.bfloat16) * 0.8)(B, S, D)),
            "shift_k": _w((lambda *s: torch.randn(*s, dtype=torch.bfloat16) * 0.8)(D, ks) * 0.5),
            "ssm_k": _w((lambda *s: torch.randn(*s, dtype=torch.bfloat16) * 1.6)(D, S) * (1.0 / S ** 0.5)),
            "ssm_D": _w((lambda *s: torch.randn(*s, dtype=torch.bfloat16) * 0.8)(D)),
        }
    _A_in = _A_setup()

    def _B_setup():
        B, S, Dm, Df = 1, 2048, 512, 1376
        return {
            "x": _w((lambda *s: torch.randn(*s, dtype=torch.bfloat16) * 0.05)(B, S, Dm)),
            "w_gate": _w((lambda *s: torch.randn(*s, dtype=torch.bfloat16) * 0.2)(Dm, Df) * (Dm ** -0.5)),
            "w_up": _w((lambda *s: torch.randn(*s, dtype=torch.bfloat16) * 0.2)(Dm, Df) * (Dm ** -0.5)),
            "w_down": _w((lambda *s: torch.randn(*s, dtype=torch.bfloat16) * 0.2)(Df, Dm) * (Df ** -0.5)),
        }
    _B_in = _B_setup()

    return {
        "A_q": _A_in["q"],
        "A_k": _A_in["k"],
        "A_v": _A_in["v"],
        "A_shift_k": _A_in["shift_k"],
        "A_ssm_k": _A_in["ssm_k"],
        "A_ssm_D": _A_in["ssm_D"],
        "B_x": _B_in["x"],
        "B_w_gate": _B_in["w_gate"],
        "B_w_up": _B_in["w_up"],
        "B_w_down": _B_in["w_down"],
    }

def reference(A_q, A_k, A_v, A_shift_k, A_ssm_k, A_ssm_D, B_x, B_w_gate, B_w_up, B_w_down):
    def _A_ref(q, k, v, shift_k, ssm_k, ssm_D):
        B, S, D = q.shape
        ks = shift_k.shape[-1]
        k_pad = _pad(k, ((0, 0), (ks - 1, 0), (0, 0)))
        k_shift = torch.zeros_like(k)
        for t in range(ks):
            k_shift = k_shift + k_pad[:, t:t + S, :] * shift_k[:, ks - 1 - t][None, None, :]
        kv = k_shift * v
        L = 2 * S
        kv_pad = _pad(kv, ((0, 0), (0, S), (0, 0)))
        kf_pad = _pad(ssm_k, ((0, 0), (0, S)))
        kv_f = (lambda x, axes=(-2,-1): torch.fft.rfft2(x.float(), dim=axes))(kv_pad[:, None, :, :], axes=(1, 2))
        kf_f = (lambda x, axes=(-2,-1): torch.fft.rfft2(x.float(), dim=axes))(kf_pad[:, None, :], axes=(1, 2))
        kf_f = (lambda x, *d: x.permute(*d))(kf_f, 1, 2, 0)
        y_f = kv_f * kf_f[None, :, :, :]
        y = (lambda x, s, axes=(-2,-1): torch.fft.irfft2(x, s=s, dim=axes).to(torch.bfloat16))(y_f, (1, L), axes=(1, 2))[:, 0, :S, :]
        y = y + kv * ssm_D[None, None, :]
        return q * y

    def _B_ref(x, w_gate, w_up, w_down):
        g = torch.einsum("bsd,df->bsf", x, w_gate)
        u = torch.einsum("bsd,df->bsf", x, w_up)
        h = _gelu(g) * u
        return torch.einsum("bsf,fd->bsd", h, w_down)

    x = A_q
    xn = _layernorm(x, axis=-1)
    s = _A_ref(xn, A_k, A_v, A_shift_k, A_ssm_k, A_ssm_D)
    g = _silu(_B_ref(xn, B_w_gate, B_w_up, B_w_down))
    return x + s * g
```

```cpp
#include <hip/hip_runtime.h>
#include <hip/hip_bf16.h>
#include <stdint.h>

typedef short bf16x8 __attribute__((ext_vector_type(8)));
typedef short s16x4 __attribute__((ext_vector_type(4)));
typedef float f32x4 __attribute__((ext_vector_type(4)));
typedef float f32x2 __attribute__((ext_vector_type(2)));
typedef unsigned v4u __attribute__((ext_vector_type(4)));
typedef unsigned v2u __attribute__((ext_vector_type(2)));
typedef __bf16 bf2 __attribute__((ext_vector_type(2)));
#define LAS __attribute__((address_space(3)))
#define DI __device__ __forceinline__

constexpr int S = 2048, D = 512, F = 1376;

DI float bf2f(unsigned short b) { return __builtin_bit_cast(float, (unsigned)b << 16); }
DI float lo(unsigned u) { return __builtin_bit_cast(float, u << 16); }
DI float hi(unsigned u) { return __builtin_bit_cast(float, u & 0xffff0000u); }
DI unsigned pk(float a, float b) { f32x2 f = {a, b}; return __builtin_bit_cast(unsigned, __builtin_convertvector(f, bf2)); }
DI float rbf(float a) { return lo(pk(a, 0.f) & 0xffffu); }

__global__ __launch_bounds__(256) void k_prep(const unsigned short* x, const unsigned short* k, const unsigned short* v,
                                              const unsigned short* shk, unsigned short* xn, unsigned short* kvT) {
    __shared__ unsigned short tile[64][66];
    const int tid = threadIdx.x, lane = tid & 63, w = tid >> 6, b = blockIdx.x;
    for (int rr = 0; rr < 2; ++rr) {
        const int row = b * 8 + w * 2 + rr;
        v4u xv = *(const v4u*)(x + (size_t)row * D + lane * 8);
        float f[8] = {lo(xv.x), hi(xv.x), lo(xv.y), hi(xv.y), lo(xv.z), hi(xv.z), lo(xv.w), hi(xv.w)};
        float s = 0.f;
        for (int j = 0; j < 8; ++j) s += f[j];
        for (int m = 32; m >= 1; m >>= 1) s += __shfl_xor(s, m);
        const float mean = s * (1.f / D);
        float q = 0.f;
        for (int j = 0; j < 8; ++j) { float d = f[j] - mean; q += d * d; }
        for (int m = 32; m >= 1; m >>= 1) q += __shfl_xor(q, m);
        const float mb = rbf(mean), vb = rbf(q * (1.f / D));
        const float den = rbf(sqrtf(rbf(vb + 1e-5f)));
        float o[8];
        for (int j = 0; j < 8; ++j) o[j] = rbf(f[j] - mb) / den;
        v4u ov = {pk(o[0], o[1]), pk(o[2], o[3]), pk(o[4], o[5]), pk(o[6], o[7])};
        *(v4u*)(xn + (size_t)row * D + lane * 8) = ov;
    }
    const int t0 = (b >> 3) * 64, c0 = (b & 7) * 64;
    for (int e = tid; e < 64 * 64; e += 256) {
        const int tt = e >> 6, cc = e & 63, t = t0 + tt, c = c0 + cc;
        float acc = 0.f;
        for (int j = 3; j >= 0; --j) {
            const float kk = (t - j >= 0) ? bf2f(k[(size_t)(t - j) * D + c]) : 0.f;
            acc = rbf(acc + rbf(kk * bf2f(shk[c * 4 + j])));
        }
        tile[cc][tt] = (unsigned short)(pk(acc * bf2f(v[(size_t)t * D + c]), 0.f) & 0xffffu);
    }
    __syncthreads();
    for (int e = tid; e < 64 * 64; e += 256) {
        const int cc = e >> 6, tt = e & 63;
        kvT[(size_t)(c0 + cc) * S + t0 + tt] = tile[cc][tt];
    }
}

constexpr int KCOPY = 4096 + 64 + 64;
constexpr int UPAD = 512;
__global__ __launch_bounds__(256) void k_conv(const unsigned short* kvT, const unsigned short* ssk, const unsigned short* ssd, unsigned short* yT) {
    __shared__ __attribute__((aligned(16))) char smem[2 * (4 * KCOPY) + 2 * (UPAD + 4096) + 4 * 8192];
    const int tid = threadIdx.x, lane = tid & 63, w = tid >> 6, ch = w >> 1, half = w & 1;
    char* kc = smem + ch * 4 * KCOPY;
    char* us = smem + 2 * 4 * KCOPY + ch * (UPAD + 4096);
    float* red = (float*)(smem + 2 * 4 * KCOPY + 2 * (UPAD + 4096));
    const int c = blockIdx.x * 2 + ch;
    const int t2 = tid & 127;
    for (int p = t2 - 16; p < 2048 + 16; p += 128)
        for (int s = 0; s < 4; ++s) {
            const int i = p + s;
            *(unsigned short*)(kc + s * KCOPY + 32 + 2 * p) = (i >= 0 && i < 2048) ? ssk[(size_t)c * S + i] : (unsigned short)0;
        }
    for (int p = t2 - 256; p < 2048; p += 128) {
        const int src = (p & ~7) + 7 - (p & 7);
        *(unsigned short*)(us + UPAD + 2 * p) = p >= 0 ? kvT[(size_t)c * S + src] : (unsigned short)0;
    }
    __syncthreads();
    const int b = lane & 15, g = lane >> 4;
    f32x4 acc[8];
    for (int n = 0; n < 8; ++n) acc[n] = (f32x4){0.f, 0.f, 0.f, 0.f};
    for (int r = 0; r < 4; ++r) {
        const int rho = half * 8 + 2 * r;
        bf16x8 P[8], Q[8];
        const int o0 = 16 * rho + 16 * (g >> 1) - 8 * (g & 1) + b - 7;
        const int s = o0 & 3;
        const char* pa = kc + s * KCOPY + 32 + 2 * (o0 - s);
        const char* pb = us + UPAD + 2 * (16 * (b - rho - (g >> 1)) + 8 * (g & 1));
#pragma unroll
        for (int i = 0; i < 8; ++i) {
            s16x4 a0 = *(const s16x4*)(pa + 512 * i), a1 = *(const s16x4*)(pa + 512 * i + 8);
            P[i] = __builtin_shufflevector(a0, a1, 0, 1, 2, 3, 4, 5, 6, 7);
            Q[i] = *(const bf16x8*)(pb + 512 * i);
        }
#pragma unroll
        for (int i = 0; i < 8; ++i)
#pragma unroll
            for (int n = i; n < 8; ++n) acc[n] = __builtin_amdgcn_mfma_f32_16x16x32_bf16(P[i], Q[n - i], acc[n], 0, 0, 0);
    }
#pragma unroll
    for (int n = 0; n < 8; ++n) *(f32x4*)(red + w * 2048 + 256 * n + 16 * b + 4 * g) = acc[n];
    __syncthreads();
    const float dd = bf2f(ssd[c]);
    for (int t = t2; t < 2048; t += 128) {
        const float y = rbf(red[(2 * ch) * 2048 + t] + red[(2 * ch + 1) * 2048 + t]);
        const int src = (t & ~7) + 7 - (t & 7);
        const float kv = bf2f(*(const unsigned short*)(us + UPAD + 2 * src));
        yT[(size_t)c * S + t] = (unsigned short)(pk(y + rbf(kv * dd), 0.f) & 0xffffu);
    }
}

DI float gelu_tanh(float x) {
    const float u = 0.7978845608028654f * (x + 0.044715f * x * x * x);
    const float e = __expf(2.f * u);
    const float th = 1.f - 2.f / (e + 1.f);
    return 0.5f * x * (1.f + th);
}

__global__ __launch_bounds__(256) void k_gemm1(const unsigned short* xn, const unsigned short* wg, const unsigned short* wu, unsigned short* h) {
    __shared__ __attribute__((aligned(16))) char sx[128 * (128 + 16)];
    __shared__ __attribute__((aligned(16))) char sw[2][64 * 64];
    const int tid = threadIdx.x, lane = tid & 63, w = tid >> 6;
    const int f0 = blockIdx.x * 32, t0 = blockIdx.y * 128;
    f32x4 acc[2][4];
    for (int i = 0; i < 2; ++i) for (int j = 0; j < 4; ++j) acc[i][j] = (f32x4){0.f, 0.f, 0.f, 0.f};
    const int c16 = lane & 15, g = lane >> 4, q = (lane & 15) >> 2, p = lane & 3;
    for (int k0 = 0; k0 < D; k0 += 64) {
        for (int e = tid; e < 128 * 8; e += 256) { const int r = e >> 3, c = e & 7; *(v4u*)(sx + r * 144 + c * 16) = *(const v4u*)(xn + (size_t)(t0 + r) * D + k0 + c * 8); }
        { const int r = tid >> 2, c = tid & 3;
          *(v4u*)(sw[0] + r * 64 + c * 16) = *(const v4u*)(wg + (size_t)(k0 + r) * F + f0 + c * 8);
          *(v4u*)(sw[1] + r * 64 + c * 16) = *(const v4u*)(wu + (size_t)(k0 + r) * F + f0 + c * 8); }
        __syncthreads();
#pragma unroll
        for (int ks = 0; ks < 2; ++ks) {
            bf16x8 A[4], B[2];
#pragma unroll
            for (int m = 0; m < 4; ++m) {
                const char* base = sw[m >> 1] + (ks * 32 + 8 * g + q) * 64 + (m & 1) * 32 + p * 8;
                s16x4 a0 = __builtin_amdgcn_ds_read_tr16_b64_v4i16((LAS s16x4*)base);
                s16x4 a1 = __builtin_amdgcn_ds_read_tr16_b64_v4i16((LAS s16x4*)(base + 4 * 64));
                A[m] = __builtin_shufflevector(a0, a1, 0, 1, 2, 3, 4, 5, 6, 7);
            }
#pragma unroll
            for (int n = 0; n < 2; ++n) B[n] = *(const bf16x8*)(sx + (w * 32 + n * 16 + c16) * 144 + ks * 64 + g * 16);
#pragma unroll
            for (int n = 0; n < 2; ++n)
#pragma unroll
                for (int m = 0; m < 4; ++m) acc[n][m] = __builtin_amdgcn_mfma_f32_16x16x32_bf16(A[m], B[n], acc[n][m], 0, 0, 0);
        }
        __syncthreads();
    }
#pragma unroll
    for (int n = 0; n < 2; ++n)
#pragma unroll
        for (int ft = 0; ft < 2; ++ft) {
            float o[4];
#pragma unroll
            for (int r = 0; r < 4; ++r) o[r] = rbf(gelu_tanh(rbf(acc[n][ft][r]))) * rbf(acc[n][2 + ft][r]);
            v2u ov = {pk(o[0], o[1]), pk(o[2], o[3])};
            *(v2u*)(h + (size_t)(t0 + w * 32 + n * 16 + c16) * F + f0 + ft * 16 + 4 * g) = ov;
        }
}

__global__ __launch_bounds__(256) void k_gemm2(const unsigned short* h, const unsigned short* wd, const unsigned short* x, const unsigned short* xn,
                                               const unsigned short* yT, unsigned short* out) {
    __shared__ __attribute__((aligned(16))) char sh[64 * (64 + 16)];
    __shared__ __attribute__((aligned(16))) char sw[32 * 128];
    const int tid = threadIdx.x, lane = tid & 63, w = tid >> 6;
    const int d0 = blockIdx.x * 64, t0 = blockIdx.y * 64;
    f32x4 acc[4];
    for (int j = 0; j < 4; ++j) acc[j] = (f32x4){0.f, 0.f, 0.f, 0.f};
    const int c16 = lane & 15, g = lane >> 4, q = (lane & 15) >> 2, p = lane & 3;
    for (int k0 = 0; k0 < F; k0 += 32) {
        { const int r = tid >> 2, c = tid & 3; *(v4u*)(sh + r * 80 + c * 16) = *(const v4u*)(h + (size_t)(t0 + r) * F + k0 + c * 8); }
        { const int r = tid >> 3, c = tid & 7; *(v4u*)(sw + r * 128 + c * 16) = *(const v4u*)(wd + (size_t)(k0 + r) * D + d0 + c * 8); }
        __syncthreads();
        bf16x8 B = *(const bf16x8*)(sh + (w * 16 + c16) * 80 + g * 16);
#pragma unroll
        for (int m = 0; m < 4; ++m) {
            const char* base = sw + (8 * g + q) * 128 + m * 32 + p * 8;
            s16x4 a0 = __builtin_amdgcn_ds_read_tr16_b64_v4i16((LAS s16x4*)base);
            s16x4 a1 = __builtin_amdgcn_ds_read_tr16_b64_v4i16((LAS s16x4*)(base + 4 * 128));
            bf16x8 A = __builtin_shufflevector(a0, a1, 0, 1, 2, 3, 4, 5, 6, 7);
            acc[m] = __builtin_amdgcn_mfma_f32_16x16x32_bf16(A, B, acc[m], 0, 0, 0);
        }
        __syncthreads();
    }
    const int t = t0 + w * 16 + c16;
#pragma unroll
    for (int m = 0; m < 4; ++m) {
        const int d = d0 + m * 16 + 4 * g;
        v2u xv = *(const v2u*)(x + (size_t)t * D + d), nv = *(const v2u*)(xn + (size_t)t * D + d);
        float xf[4] = {lo(xv.x), hi(xv.x), lo(xv.y), hi(xv.y)}, nf[4] = {lo(nv.x), hi(nv.x), lo(nv.y), hi(nv.y)};
        float o[4];
#pragma unroll
        for (int r = 0; r < 4; ++r) {
            const float y = bf2f(yT[(size_t)(d + r) * S + t]);
            const float m1 = rbf(acc[m][r]);
            const float gg = rbf(m1 / (1.f + __expf(-m1)));
            o[r] = xf[r] + rbf(rbf(nf[r] * y) * gg);
        }
        v2u ov = {pk(o[0], o[1]), pk(o[2], o[3])};
        *(v2u*)(out + (size_t)t * D + d) = ov;
    }
}

extern "C" void kernel_launch(void* const* d_in, const int* in_sizes, int n_in, void* d_out, int out_size, void* d_ws, size_t ws_size,
                              hipStream_t stream) {
    const unsigned short* x = (const unsigned short*)d_in[0];
    const unsigned short* k = (const unsigned short*)d_in[1];
    const unsigned short* v = (const unsigned short*)d_in[2];
    const unsigned short* shk = (const unsigned short*)d_in[3];
    const unsigned short* ssk = (const unsigned short*)d_in[4];
    const unsigned short* ssd = (const unsigned short*)d_in[5];
    const unsigned short* wg = (const unsigned short*)d_in[7];
    const unsigned short* wu = (const unsigned short*)d_in[8];
    const unsigned short* wd = (const unsigned short*)d_in[9];
    char* ws = (char*)d_ws + 65536;
    unsigned short* xn = (unsigned short*)ws;
    unsigned short* kvT = (unsigned short*)(ws + (2 << 20));
    unsigned short* yT = (unsigned short*)(ws + (4 << 20));
    unsigned short* h = (unsigned short*)(ws + (6 << 20));
    k_prep<<<256, 256, 0, stream>>>(x, k, v, shk, xn, kvT);
    k_conv<<<256, 256, 0, stream>>>(kvT, ssk, ssd, yT);
    k_gemm1<<<dim3(43, 16), 256, 0, stream>>>(xn, wg, wu, h);
    k_gemm2<<<dim3(8, 32), 256, 0, stream>>>(h, wd, x, xn, yT, (unsigned short*)d_out);
}
```

```cpp
#define EPI_PK
#define PF_CODE 0
#include <hip/hip_runtime.h>
#include <stdint.h>

typedef short bf16x8 __attribute__((ext_vector_type(8)));
typedef short s16x4 __attribute__((ext_vector_type(4)));
typedef float f32x2 __attribute__((ext_vector_type(2)));
typedef float f32x4 __attribute__((ext_vector_type(4)));
typedef float f32x16 __attribute__((ext_vector_type(16)));
typedef unsigned v2u __attribute__((ext_vector_type(2)));
typedef unsigned v4u __attribute__((ext_vector_type(4)));
typedef __bf16 bf2 __attribute__((ext_vector_type(2)));
typedef unsigned long long u64;
typedef unsigned short u16;
#define GAS __attribute__((address_space(1)))
#define LAS __attribute__((address_space(3)))
#define DI __device__ __forceinline__
#define RLX_AGENT __ATOMIC_RELAXED, __HIP_MEMORY_SCOPE_AGENT
#define MEMBAR() asm volatile("" ::: "memory")
#define WAIT_VM(n) asm volatile("s_waitcnt vmcnt(" #n ")" ::: "memory")
#define SCHED() __builtin_amdgcn_sched_barrier(0)
#define UNLIKELY(x) __builtin_expect(!!(x), 0)

extern "C" __device__ u64 llvm_dispatch_id() __asm("llvm.amdgcn.dispatch.id");

constexpr int S = 2048, D = 512, F = 1376;
constexpr int SLOT = 40992, GOFF = 16384, UOFF = 28704;
constexpr int KC = 3 * SLOT, KCP = 4160, KCH = 16640;
constexpr int RED = 94208, UB = 110592, UCH = 4608;
constexpr int PART = 24576, PPITCH = 272;
constexpr int YST = 151552, CS = 159744, ST = 161280, FLG = 161792, FRP = 162048, LDS_BYTES = 163840;
constexpr int DUMMY = 159456;
constexpr int CSG = KC, CSU = CSG + 6144 + 32, KIM = KC + 12416, SHI = KIM + 9216, VIM = SHI + 512, STG = VIM + 8192 - 2048;
enum { L_FULL = 0  , L_WDF = 9, L_TAG = 10, L_PRE = 12, L_SV = 16, L_CV1 = 20, L_CV2 = 24,
       L_FYCNT = 28, L_F0CNT = 29, L_HGO = 30, L_CGO = 31, L_YT = 32, L_WDC = 36, L_PART = 40, L_CVD = 44, L_CF = 48, L_GO = 52 };
constexpr size_t ROT0 = 2u << 20, ROTS = 14u << 20;
#define NROT_ 16
#define HWIN 14
#define SVC_GATE 0
constexpr int NROT = NROT_;
constexpr int W_STAT = 0, W_CSP = 32768  , W_IN0 = 1 << 19, W_INH = 3 << 19, W_INY = 4 << 19, W_KVT = 5 << 19, W_YT = 9 << 19,
              W_H = 13 << 19, W_BYTES = W_H + 64 * 86 * 1024;
constexpr unsigned LSPIN = 1u << 22, GSPIN = 1u << 18;

#define STAMP(i) do {} while (0)
#define STAMP_INIT() do {} while (0)
#define STAMP_FLUSH() do {} while (0)
#define PRIO_G 2
#define PRIO_C 3
#define PRIO_L 1
#define ROTF rfl(ROT_ON ? ((j & 7) + (3 * (j >> 3)) / 2) % 6 : (b >> 10))

#define FREE0 0u
#define CF0 cst
#define PF_G1 0
#define PF_DELAY 8
#define PF_WD 1
#define HROT (5 * ct)
#define WSTEP(i) ((i) + hrot >= 43 ? (i) + hrot - 43 : (i) + hrot)
#define POLL_SLEEP 2
#define SWEEP_AT 4
#define ROT_ON 0
#define GO_GATE 1
#define POLL_AT 2
#define OUT_AUX 16
#define PF_TLB 0
constexpr int CODE_BYTES = 20480;
struct Params { const char *x, *wg, *wu, *k, *v, *shk, *ssk, *ssd, *wd; char *out, *ws; };

DI float asf(unsigned u) { return __builtin_bit_cast(float, u); }
DI float lo(unsigned u) { return __builtin_bit_cast(float, u << 16); }
DI float hi(unsigned u) { return __builtin_bit_cast(float, u & 0xffff0000u); }
DI unsigned pk(float a, float b) { f32x2 f = {a, b}; return __builtin_bit_cast(unsigned, __builtin_convertvector(f, bf2)); }
DI float rbf(float a) { return lo(pk(a, a)); }
DI unsigned rfl(unsigned v) { return __builtin_amdgcn_readfirstlane(v); }
DI v4u mksrd(const void* base, unsigned bytes) { const u64 a = (u64)base; return (v4u){(unsigned)a, (unsigned)(a >> 32) & 0xffffu, bytes, 0x00020000u}; }
DI __amdgpu_buffer_rsrc_t mkrs(const void* base, int bytes) { return __builtin_amdgcn_make_buffer_rsrc((void*)base, 0, bytes, 0x00020000); }
DI v4u ld16(__amdgpu_buffer_rsrc_t rs, int off) { return __builtin_amdgcn_raw_buffer_load_b128(rs, off, 0, 0); }
DI v4u ld16c(__amdgpu_buffer_rsrc_t rs, int off) { return __builtin_amdgcn_raw_buffer_load_b128(rs, off, 0, 16); }
DI void st16c(v4u v, __amdgpu_buffer_rsrc_t rs, int off) { __builtin_amdgcn_raw_buffer_store_b128(v, rs, off, 0, 16); }
DI void st8c(unsigned a, unsigned b, __amdgpu_buffer_rsrc_t rs, int off) { __builtin_amdgcn_raw_buffer_store_b64((v2u){a, b}, rs, off, 0, 16); }
DI void ald16(v4u& d, v4u srd, unsigned voff) { asm volatile("buffer_load_dwordx4 %0, %1, %2, 0 offen" : "=&v"(d) : "v"(voff), "s"(srd) : "memory"); }
DI void ald16c(v4u& d, v4u srd, unsigned voff) { asm volatile("buffer_load_dwordx4 %0, %1, %2, 0 offen sc1" : "=&v"(d) : "v"(voff), "s"(srd) : "memory"); }
DI void ald4c(unsigned& d, v4u srd, unsigned voff) { asm volatile("buffer_load_dword %0, %1, %2, 0 offen sc1" : "=&v"(d) : "v"(voff), "s"(srd) : "memory"); }
#define WAIT_TIE(n, ...) asm volatile("s_waitcnt vmcnt(" #n ")" : __VA_ARGS__ :: "memory")
DI void dma16(v4u srd, unsigned voff, unsigned soff, unsigned lds) {
    asm volatile("s_mov_b32 m0, %3\n\ts_nop 0\n\tbuffer_load_dwordx4 %0, %1, %2 offen lds" ::"v"(voff), "s"(srd), "s"(soff), "s"(lds) : "memory");
}
DI void dma16x2(v4u srd, unsigned voff, unsigned voff2, unsigned soff, unsigned lds) {
    asm volatile("s_mov_b32 m0, %4\n\ts_nop 0\n\tbuffer_load_dwordx4 %0, %2, %3 offen lds\n\tbuffer_load_dwordx4 %1, %2, %3 offen offset:1024 lds"
                 ::"v"(voff), "v"(voff2), "s"(srd), "s"(soff), "s"(lds) : "memory");
}
DI void touch(const char* a, unsigned lds) {
    asm volatile("s_mov_b32 m0, %1\n\ts_nop 0\n\tglobal_load_lds_dword %0, off" ::"v"(a), "s"(lds) : "memory");
}
DI f32x16 mfma32(bf16x8 a, bf16x8 b, f32x16 c) { return __builtin_amdgcn_mfma_f32_32x32x16_bf16(a, b, c, 0, 0, 0); }
DI f32x4 mfma16(bf16x8 a, bf16x8 b, f32x4 c) { return __builtin_amdgcn_mfma_f32_16x16x32_bf16(a, b, c, 0, 0, 0); }
DI bf16x8 trd(LAS char* a, int off2) {
    s16x4 x = __builtin_amdgcn_ds_read_tr16_b64_v4i16((LAS s16x4*)a), y = __builtin_amdgcn_ds_read_tr16_b64_v4i16((LAS s16x4*)(a + off2));
    return __builtin_shufflevector(x, y, 0, 1, 2, 3, 4, 5, 6, 7);
}
DI void lset(LAS char* L, int w, int lane, unsigned v) { MEMBAR(); if (lane == 0) *(volatile LAS unsigned*)(L + FLG + 4 * w) = v; MEMBAR(); }
DI unsigned lrd(LAS char* a) { return *(volatile LAS unsigned*)a; }
template <int SLEEP = 1> DI void lspin(LAS char* a, unsigned need) {
#pragma unroll 1
    for (unsigned s = 0; s < LSPIN; ++s) { if (__all(lrd(a) >= need)) break; __builtin_amdgcn_s_sleep(SLEEP); }
}
template <int SLEEP = 1> DI void lwait(LAS char* a, unsigned need) { if (UNLIKELY(!__all(lrd(a) >= need))) lspin<SLEEP>(a, need); MEMBAR(); }
DI void lcheck(unsigned v, LAS char* a, unsigned need) { if (UNLIKELY(!__all(v >= need))) lspin(a, need); MEMBAR(); }
#define FL(w, n) (L + FLG + 4 * ((w) + (lane & ((n) - 1))))
#define FR() (L + FRP + 256 * (lane & 3))
DI void fset(LAS char* L, int w, int lane, unsigned v) { MEMBAR(); *(volatile LAS unsigned*)(L + FRP + 256 * w + 4 * lane) = v; MEMBAR(); }
DI void gwait(const char* f, u64 tag) {
#pragma unroll 1
    for (unsigned s = 0; s < GSPIN; ++s) { if (__all(__hip_atomic_load((GAS u64*)f, RLX_AGENT) == tag)) break; __builtin_amdgcn_s_sleep(POLL_SLEEP); }
    __builtin_amdgcn_fence(__ATOMIC_ACQUIRE, "wavefront");
    MEMBAR();
}
DI bool arrive(LAS char* L, int cnt, unsigned n, int lane) {
    unsigned old = 0;
    MEMBAR();
    if (lane == 0) old = __hip_atomic_fetch_add((LAS unsigned*)(L + FLG + 4 * cnt), 1u, __ATOMIC_RELAXED, __HIP_MEMORY_SCOPE_WORKGROUP);
    return rfl(old) == n - 1;
}
DI void ladd(LAS char* L, int w, int lane) {
    MEMBAR();
    if (lane == 0) (void)__hip_atomic_fetch_add((LAS unsigned*)(L + FLG + 4 * w), 1u, __ATOMIC_RELAXED, __HIP_MEMORY_SCOPE_WORKGROUP);
    MEMBAR();
}
DI void gflag(char* f, u64 tag) { __hip_atomic_store((GAS u64*)f, tag, RLX_AGENT); }
template <int CTRL> DI float dpp_add(float v) { return v + __builtin_bit_cast(float, __builtin_amdgcn_update_dpp(0, __builtin_bit_cast(int, v), CTRL, 0xf, 0xf, true)); }
DI float sum16(float v) { v = dpp_add<0xB1>(v); v = dpp_add<0x4E>(v); v = dpp_add<0x141>(v); return dpp_add<0x140>(v); }
DI float sum32(float v) {
    v = sum16(v);
    const unsigned u = __builtin_bit_cast(unsigned, v);
    const v2u s = __builtin_amdgcn_permlane16_swap(u, u, false, false);
    return asf(s.x) + asf(s.y);
}

__global__ __launch_bounds__(768, 3) void fused_h3_geglu(Params p) {
    extern __shared__ __attribute__((aligned(16))) char smem[];
    asm volatile("fused_h3_geglu_begin:");
    LAS char* L = (LAS char*)smem;
    const unsigned Lb = (unsigned)(uintptr_t)L;
    const int lane = threadIdx.x & 63, w = rfl(threadIdx.x >> 6), b = blockIdx.x;
    const int j = b >> 3, tau = b & 1, kap = (b >> 1) & 3;
    const int pn = 8 * tau + (j & 7), cg = 4 * kap + (j >> 3);
    const int o16 = min(6 * cg, 22 + 4 * cg), n16 = cg < 11 ? 6 : 4, w16 = rfl(min(o16, 80));
    const int tt = 4 * (4 * tau + kap) + (j & 3), ct = j >> 2;
    const int hrot = HROT;
    const u64 did = llvm_dispatch_id();
    STAMP_INIT(); STAMP(0);
    if (w >= 4 && w < 8) __builtin_amdgcn_s_setprio(3);

    const unsigned vd = (lane >> 3) * 1024 + (((lane & 7) ^ (((lane >> 4) & 1) << 2)) << 4);
    if (w == 0) {
        *(volatile LAS unsigned*)(L + FLG + 4 * lane) = lane == L_FULL + 8 ? 4u : 0u;
        if (lane < 4) *(volatile LAS unsigned*)(L + FRP + 256 * lane) = FREE0;
        asm volatile("s_waitcnt lgkmcnt(0)" ::: "memory");
    }
    if (w < 4 || w >= 8) __builtin_amdgcn_s_barrier();
    if (w >= 4 && w < 8) {
        const int lq = w - 4;
        const unsigned vx0 = (lane >> 3) * 1024 + (((lane & 7) ^ (lane >> 4)) << 4), vx1 = (vx0 ^ 64) + 8192 - 1024;
        const unsigned vw0 = ((lane * 43691u) >> 19) * 2560 + lane * 16, vw1 = (((lane + 64) * 43691u) >> 19) * 2560 + lane * 16,
                       vw2 = (((lane + 128) * 43691u) >> 19) * 2560 + lane * 16;
        unsigned kc = 0;
        const unsigned rot = ROTF;
        unsigned sxo = (128 * pn + 32 * lq) * 1024, swo = w16 * 32 + (lq >> 1) * 32 * 2752, mx = Lb + lq * 4096, mw = Lb + ((lq & 1) ? UOFF : GOFF) + (lq >> 1) * 6144;
        unsigned sc = 0, spin, fa = Lb + FLG + 4 * L_FULL, need = -2, vf;
        asm volatile("" ::"v"(vx0), "v"(vx1), "v"(vw0), "v"(vw1), "v"(vw2), "s"(sxo), "s"(swo), "s"(mx), "s"(mw));
        STAMP(1);
        asm volatile(
                "Lld_loop_%=:\n\t"
                "s_mov_b32 m0, %2\n\t"
                "s_add_u32 %2, %2, 2048\n\t"
                "buffer_load_dwordx4 %10, %15, %0 offen lds\n\t"
                "buffer_load_dwordx4 %11, %15, %0 offen offset:1024 lds\n\t"
                "s_mov_b32 m0, %3\n\t"
                "s_add_u32 %3, %3, 3072\n\t"
                "buffer_load_dwordx4 %12, %16, %1 offen lds\n\t"
                "buffer_load_dwordx4 %13, %16, %1 offen offset:1024 lds\n\t"
                "buffer_load_dwordx4 %14, %16, %1 offen offset:2048 lds\n\t"
                "s_add_u32 %0, %0, 16384\n\t"
                "s_add_u32 %1, %1, 44032\n\t"
                "s_cmp_lt_u32 %4, 2\n\t"
                "s_cbranch_scc1 Lld_skip_%=\n\t"
                "s_waitcnt vmcnt(5)\n\t"
                "s_mov_b64 exec, 1\n\t"
                "ds_add_u32 %6, %18\n\t"
                "s_mov_b64 exec, -1\n\t"
                "v_add_u32 %6, 4, %6\n\t"
                "Lld_skip_%=:\n\t"
                "s_mov_b32 m0, %2\n\t"
                "s_add_u32 %2, %2, 2048\n\t"
                "buffer_load_dwordx4 %10, %15, %0 offen lds\n\t"
                "buffer_load_dwordx4 %11, %15, %0 offen offset:1024 lds\n\t"
                "s_mov_b32 m0, %3\n\t"
                "s_add_u32 %3, %3, 3072\n\t"
                "buffer_load_dwordx4 %12, %16, %1 offen lds\n\t"
                "buffer_load_dwordx4 %13, %16, %1 offen offset:1024 lds\n\t"
                "buffer_load_dwordx4 %14, %16, %1 offen offset:2048 lds\n\t"
                "s_add_u32 %0, %0, -16256\n\t"
                "s_add_u32 %1, %1, 132096\n\t"
                "ds_read_b32 %8, %17\n\t"
                "s_bitcmp1_b32 36, %4\n\t"
                "s_cselect_b32 %5, -122976, 0\n\t"
                "s_add_u32 %2, %2, %5\n\t"
                "s_add_u32 %3, %3, %5\n\t"
                "s_add_u32 %2, %2, 36896\n\t"
                "s_add_u32 %3, %3, 34848\n\t"
                "s_add_u32 %9, %9, 1\n\t"
                "s_cmp_lg_u32 %4, 1\n\t"
                "s_cbranch_scc1 Lld_norot_%=\n\t"
                "s_add_u32 %9, %9, %19\n\t"
                "s_add_u32 %0, %0, %20\n\t"
                "s_add_u32 %1, %1, %21\n\t"
                "Lld_norot_%=:\n\t"
                "s_cmp_lg_u32 %9, 8\n\t"
                "s_cbranch_scc1 Lld_nowrap_%=\n\t"
                "s_sub_u32 %0, %0, 768\n\t"
                "s_sub_u32 %1, %1, 1056768\n\t"
                "s_mov_b32 %9, 2\n\t"
                "Lld_nowrap_%=:\n\t"
                "s_add_u32 %4, %4, 1\n\t"
                "v_add_u32 %7, 1, %7\n\t"
                "s_cmp_lg_u32 %4, 1\n\t"
                "s_cbranch_scc1 Lld_not0_%=\n\t"
                "s_barrier\n\t"
                "s_setprio 1\n\t"
                "Lld_not0_%=:\n\t"
                "s_cmp_lg_u32 %4, 2\n\t"
                "s_cbranch_scc1 Lld_not1_%=\n\t"
                "s_waitcnt vmcnt(10)\n\t"
                "s_mov_b64 exec, 1\n\t"
                "ds_add_u32 %6, %18\n\t"
                "s_mov_b64 exec, -1\n\t"
                "v_add_u32 %6, 4, %6\n\t"
                "Lld_not1_%=:\n\t"
                "s_cmp_eq_u32 %4, 8\n\t"
                "s_cbranch_scc1 Lld_tail_%=\n\t"
                "s_waitcnt lgkmcnt(0)\n\t"
                "v_cmp_gt_i32 vcc, %7, %8\n\t"
                "s_cbranch_vccz Lld_loop_%=\n\t"
                "Lld_slow_%=:\n\t"
                "s_mov_b32 %5, 0x400000\n\t"
                "Lld_slows_%=:\n\t"
                "s_sleep 1\n\t"
                "ds_read_b32 %8, %17\n\t"
                "s_sub_u32 %5, %5, 1\n\t"
                "s_cbranch_scc1 Lld_loop_%=\n\t"
                "s_waitcnt lgkmcnt(0)\n\t"
                "v_cmp_gt_i32 vcc, %7, %8\n\t"
                "s_cbranch_vccnz Lld_slows_%=\n\t"
                "s_branch Lld_loop_%=\n\t"
                "Lld_tail_%=:\n\t"
            : "+s"(sxo), "+s"(swo), "+s"(mx), "+s"(mw), "+s"(sc), "=&s"(spin), "+v"(fa), "+v"(need), "=&v"(vf), "+s"(kc)
            : "v"(vx0), "v"(vx1), "v"(vw0), "v"(vw1), "v"(vw2), "s"(mksrd(p.x, S * D * 2)), "s"(mksrd((lq & 1) ? p.wu : p.wg, D * F * 2)),
              "v"(Lb + FRP + 256 * (lane & 3)), "v"(1u), "s"(rot), "s"(rot * 128), "s"(rot * 64 * 2752)
            : "memory", "vcc", "scc");
        {
            const char* a = p.wd + (344 * (j & 3) + 86 * lq + lane) * 1024 + ct * 128;
            touch(a, Lb + DUMMY);
            if (lane < 22) touch(a + 65536, Lb + DUMMY);
            WAIT_VM(2);
        }
        ladd(L, L_FULL + 7, lane);
        STAMP(2);
    }

    char* R = p.ws + ROT0 + (size_t)((unsigned)did & (NROT - 1)) * ROTS;
    const __amdgpu_buffer_rsrc_t rw = mkrs(R, W_BYTES);

    if (w < 4) {
        const int th = w & 1, fh = w >> 1, hh = lane >> 5, blk = (lane >> 4) & 1, q = (lane & 15) >> 2, pp = lane & 3;
        f32x16 acc[2][3];
#pragma unroll
        for (int a = 0; a < 2; ++a)
#pragma unroll
            for (int u = 0; u < 3; ++u)
#pragma unroll
                for (int i = 0; i < 16; ++i) acc[a][u][i] = 0.f;
        {
            unsigned xa0 = Lb + (64 * th + (lane & 31)) * 128 + ((hh ^ (((lane & 31) >> 1) & 7)) << 4), xa1 = xa0 ^ 32, xa2 = xa0 ^ 64, xa3 = xa0 ^ 96;
            unsigned wa = Lb + (blk ? UOFF : GOFF) + (8 * hh + q) * 192 + 96 * fh + 8 * pp, fa = Lb + FLG + 4 * (L_FULL + 1), cnt = 0, vf;
            unsigned sc = 0, dl, spin;
            asm volatile("" : "+v"(acc[0][0]), "+v"(acc[1][0]), "+v"(acc[0][1]), "+v"(acc[1][1]), "+v"(acc[0][2]), "+v"(acc[1][2]), "+v"(xa0), "+v"(xa1),
                         "+v"(xa2), "+v"(xa3), "+v"(wa));
            __builtin_amdgcn_s_setprio(PRIO_G);
            lspin(FL(L_FULL, 1), 4);
            STAMP(1);
            asm volatile(
                "s_waitcnt lgkmcnt(0)\n\t"
                "ds_read_b128 v[128:131], %6\n\t"
                "ds_read_b64_tr_b16 v[136:137], %10 offset:0\n\t"
                "ds_read_b64_tr_b16 v[138:139], %10 offset:768\n\t"
                "ds_read_b128 v[132:135], %6 offset:4096\n\t"
                "ds_read_b64_tr_b16 v[140:141], %10 offset:32\n\t"
                "ds_read_b64_tr_b16 v[142:143], %10 offset:800\n\t"
                "ds_read_b64_tr_b16 v[144:145], %10 offset:64\n\t"
                "ds_read_b64_tr_b16 v[146:147], %10 offset:832\n\t"
                "Lg1_loop_%=:\n\t"
                "s_waitcnt lgkmcnt(5)\n\t"
                "v_mfma_f32_32x32x16_bf16 %0, v[136:139], v[128:131], %0\n\t"
                "ds_read_b128 v[148:151], %7\n\t"
                "ds_read_b64_tr_b16 v[156:157], %10 offset:3072\n\t"
                "s_waitcnt lgkmcnt(6)\n\t"
                "v_mfma_f32_32x32x16_bf16 %1, v[136:139], v[132:135], %1\n\t"
                "ds_read_b64_tr_b16 v[158:159], %10 offset:3840\n\t"
                "ds_read_b128 v[152:155], %7 offset:4096\n\t"
                "s_waitcnt lgkmcnt(6)\n\t"
                "v_mfma_f32_32x32x16_bf16 %2, v[140:143], v[128:131], %2\n\t"
                "ds_read_b64_tr_b16 v[160:161], %10 offset:3104\n\t"
                "ds_read_b64_tr_b16 v[162:163], %10 offset:3872\n\t"
                "v_mfma_f32_32x32x16_bf16 %3, v[140:143], v[132:135], %3\n\t"
                "ds_read_b64_tr_b16 v[164:165], %10 offset:3136\n\t"
                "ds_read_b64_tr_b16 v[166:167], %10 offset:3904\n\t"
                "s_waitcnt lgkmcnt(8)\n\t"
                "v_mfma_f32_32x32x16_bf16 %4, v[144:147], v[128:131], %4\n\t"
                "s_bitcmp1_b32 36, %14\n\t"
                "s_cselect_b32 %15, -81984, %18\n\t"
                "v_add_u32 %12, 1, %12\n\t"
                "v_mfma_f32_32x32x16_bf16 %5, v[144:147], v[132:135], %5\n\t"
                "v_add_u32 %6, %15, %6\n\t"
                "v_add_u32 %7, %15, %7\n\t"
                "s_waitcnt lgkmcnt(5)\n\t"
                "v_mfma_f32_32x32x16_bf16 %0, v[156:159], v[148:151], %0\n\t"
                "ds_read_b128 v[128:131], %8\n\t"
                "ds_read_b64_tr_b16 v[136:137], %10 offset:6144\n\t"
                "s_waitcnt lgkmcnt(6)\n\t"
                "v_mfma_f32_32x32x16_bf16 %1, v[156:159], v[152:155], %1\n\t"
                "ds_read_b64_tr_b16 v[138:139], %10 offset:6912\n\t"
                "ds_read_b128 v[132:135], %8 offset:4096\n\t"
                "s_waitcnt lgkmcnt(6)\n\t"
                "v_mfma_f32_32x32x16_bf16 %2, v[160:163], v[148:151], %2\n\t"
                "ds_read_b64_tr_b16 v[140:141], %10 offset:6176\n\t"
                "ds_read_b64_tr_b16 v[142:143], %10 offset:6944\n\t"
                "v_mfma_f32_32x32x16_bf16 %3, v[160:163], v[152:155], %3\n\t"
                "ds_read_b64_tr_b16 v[144:145], %10 offset:6208\n\t"
                "ds_read_b64_tr_b16 v[146:147], %10 offset:6976\n\t"
                "s_waitcnt lgkmcnt(8)\n\t"
                "v_mfma_f32_32x32x16_bf16 %4, v[164:167], v[148:151], %4\n\t"
                "v_add_u32 %8, %15, %8\n\t"
                "v_mfma_f32_32x32x16_bf16 %5, v[164:167], v[152:155], %5\n\t"
                "ds_read_b32 %13, %11\n\t"
                "s_waitcnt lgkmcnt(6)\n\t"
                "v_mfma_f32_32x32x16_bf16 %0, v[136:139], v[128:131], %0\n\t"
                "ds_read_b128 v[148:151], %9\n\t"
                "ds_read_b64_tr_b16 v[156:157], %10 offset:9216\n\t"
                "s_waitcnt lgkmcnt(7)\n\t"
                "v_mfma_f32_32x32x16_bf16 %1, v[136:139], v[132:135], %1\n\t"
                "ds_read_b64_tr_b16 v[158:159], %10 offset:9984\n\t"
                "ds_read_b128 v[152:155], %9 offset:4096\n\t"
                "s_waitcnt lgkmcnt(7)\n\t"
                "v_mfma_f32_32x32x16_bf16 %2, v[140:143], v[128:131], %2\n\t"
                "ds_read_b64_tr_b16 v[160:161], %10 offset:9248\n\t"
                "ds_read_b64_tr_b16 v[162:163], %10 offset:10016\n\t"
                "v_mfma_f32_32x32x16_bf16 %3, v[140:143], v[132:135], %3\n\t"
                "ds_read_b64_tr_b16 v[164:165], %10 offset:9280\n\t"
                "ds_read_b64_tr_b16 v[166:167], %10 offset:10048\n\t"
                "s_waitcnt lgkmcnt(8)\n\t"
                "v_mfma_f32_32x32x16_bf16 %4, v[144:147], v[128:131], %4\n\t"
                "ds_write_b32 %17, %12\n\t"
                "v_add_u32 %9, %15, %9\n\t"
                "v_add_u32 %10, %15, %10\n\t"
                "v_cmp_gt_u32 vcc, 4, %13\n\t"
                "v_mfma_f32_32x32x16_bf16 %5, v[144:147], v[132:135], %5\n\t"
                "s_cbranch_vccnz Lg1_slow_%=\n\t"
                "Lg1_back_%=:\n\t"
                "s_waitcnt lgkmcnt(6)\n\t"
                "v_mfma_f32_32x32x16_bf16 %0, v[156:159], v[148:151], %0\n\t"
                "ds_read_b128 v[128:131], %6\n\t"
                "ds_read_b64_tr_b16 v[136:137], %10 offset:0\n\t"
                "s_waitcnt lgkmcnt(7)\n\t"
                "v_mfma_f32_32x32x16_bf16 %1, v[156:159], v[152:155], %1\n\t"
                "ds_read_b64_tr_b16 v[138:139], %10 offset:768\n\t"
                "ds_read_b128 v[132:135], %6 offset:4096\n\t"
                "s_waitcnt lgkmcnt(7)\n\t"
                "v_mfma_f32_32x32x16_bf16 %2, v[160:163], v[148:151], %2\n\t"
                "ds_read_b64_tr_b16 v[140:141], %10 offset:32\n\t"
                "ds_read_b64_tr_b16 v[142:143], %10 offset:800\n\t"
                "v_mfma_f32_32x32x16_bf16 %3, v[160:163], v[152:155], %3\n\t"
                "ds_read_b64_tr_b16 v[144:145], %10 offset:64\n\t"
                "ds_read_b64_tr_b16 v[146:147], %10 offset:832\n\t"
                "s_waitcnt lgkmcnt(9)\n\t"
                "v_mfma_f32_32x32x16_bf16 %4, v[164:167], v[148:151], %4\n\t"
                "v_add_u32 %11, 4, %11\n\t"
                "s_add_u32 %14, %14, 1\n\t"
                "s_cmp_lt_u32 %14, 8\n\t"
                "v_mfma_f32_32x32x16_bf16 %5, v[164:167], v[152:155], %5\n\t"
                "s_cbranch_scc1 Lg1_loop_%=\n\t"
                "s_branch Lg1_done_%=\n\t"
                "Lg1_slow_%=:\n\t"
                "s_mov_b32 %16, 0x400000\n\t"
                "Lg1_slows_%=:\n\t"
                "ds_read_b32 %13, %11\n\t"
                "s_sub_u32 %16, %16, 1\n\t"
                "s_cbranch_scc1 Lg1_back_%=\n\t"
                "s_waitcnt lgkmcnt(0)\n\t"
                "v_cmp_gt_u32 vcc, 4, %13\n\t"
                "s_cbranch_vccnz Lg1_slows_%=\n\t"
                "s_branch Lg1_back_%=\n\t"
                "Lg1_done_%=:\n\t"
                "s_waitcnt lgkmcnt(0)\n\t"
                "s_nop 15\n\t"
                : "+v"(acc[0][0]), "+v"(acc[1][0]), "+v"(acc[0][1]), "+v"(acc[1][1]), "+v"(acc[0][2]), "+v"(acc[1][2]), "+v"(xa0), "+v"(xa1), "+v"(xa2),
                  "+v"(xa3), "+v"(wa), "+v"(fa), "+v"(cnt), "=&v"(vf), "+s"(sc), "=&s"(dl), "=&s"(spin)
                : "v"(Lb + FRP + 256 * w + 4 * lane), "s"(SLOT)
                : "memory", "vcc", "scc", "v128", "v129", "v130", "v131", "v132", "v133", "v134", "v135", "v136", "v137", "v138", "v139", "v140", "v141", "v142", "v143", "v144", "v145", "v146", "v147", "v148", "v149", "v150", "v151", "v152", "v153", "v154", "v155", "v156", "v157", "v158", "v159", "v160", "v161", "v162", "v163", "v164", "v165", "v166", "v167");
        }
        STAMP(2);
        unsigned st[2]; v2u cs[3], tg2;
#pragma unroll 1
        for (unsigned s = 0; s < LSPIN; ++s) {
            const unsigned f = lrd(FL(L_PRE, 2));
            tg2 = *(volatile LAS v2u*)(L + FLG + 4 * L_TAG);
#pragma unroll
            for (int a = 0; a < 2; ++a) st[a] = lrd(L + ST + 4 * (64 * th + 32 * a + (lane & 31)));
#pragma unroll
            for (int u = 0; u < 3; ++u) cs[u] = *(volatile LAS v2u*)(L + CS + (3 * fh + u) * 256 + (lane & 31) * 8);
            if (__all(f >= 1)) break;
        }
        const u64 tag = (u64)tg2.x | ((u64)tg2.y << 32);
        STAMP(3);
#pragma unroll
        for (int u = 0; u < 3; ++u)
#pragma unroll
            for (int a = 0; a < 2; ++a) {
                const unsigned nm = hh ? 0u : ((st[a] & 0xffffu) ^ 0x8000u);
                acc[a][u] = mfma32(__builtin_bit_cast(bf16x8, (v4u){cs[u].x, cs[u].y, 0u, 0u}), __builtin_bit_cast(bf16x8, (v4u){nm | (nm << 16), nm, 0u, 0u}), acc[a][u]);
            }
        float k1[2], k2[2], d2[2];
#pragma unroll
        for (int a = 0; a < 2; ++a) {
            const float den = hi(st[a]), rs = __builtin_amdgcn_rcpf(den);
            k1[a] = -2.302208198f * rs; k2[a] = -0.1029432397f * rs * rs * rs; d2[a] = den * den;
        }
        SCHED();
#pragma unroll
        for (int u = 0; u < 3; ++u) {
            const int U = w16 + 3 * fh + u;
            if (U >= o16 && U < o16 + n16) {
#pragma unroll
                for (int a = 0; a < 2; ++a) {
                    f32x2 g[4], m[4], e[4];
#pragma unroll
                    for (int i = 0; i < 4; ++i) { g[i] = (f32x2){acc[a][u][2 * i], acc[a][u][2 * i + 1]}; e[i] = g[i] * g[i]; }
#pragma unroll
                    for (int i = 0; i < 4; ++i) e[i] = __builtin_elementwise_fma(e[i], (f32x2){k2[a], k2[a]}, (f32x2){k1[a], k1[a]});
#pragma unroll
                    for (int i = 0; i < 4; ++i) e[i] *= g[i];
#pragma unroll
                    for (int i = 0; i < 4; ++i) {
                        e[i] = (f32x2){__builtin_amdgcn_exp2f(e[i].x), __builtin_amdgcn_exp2f(e[i].y)};
                        m[i] = g[i] * (f32x2){acc[a][u][8 + 2 * i], acc[a][u][9 + 2 * i]};
                    }
#pragma unroll
                    for (int i = 0; i < 4; ++i) e[i] = __builtin_elementwise_fma(e[i], (f32x2){d2[a], d2[a]}, (f32x2){d2[a], d2[a]});
#pragma unroll
                    for (int i = 0; i < 4; ++i) m[i] *= (f32x2){__builtin_amdgcn_rcpf(e[i].x), __builtin_amdgcn_rcpf(e[i].y)};
                    const v4u hv = {pk(m[0].x, m[0].y), pk(m[1].x, m[1].y), pk(m[2].x, m[2].y), pk(m[3].x, m[3].y)};
                    st16c(hv, rw, W_H + ((4 * pn + 2 * th + a) * 86 + U) * 1024 + lane * 16);
                }
            }
        }
        fset(L, w, lane, 9);
        STAMP(4);
        WAIT_VM(0);
        if (lane < 8) gflag(R + W_INH + (((2 * pn + th) * 8 + lane) * 32 + 2 * cg + fh) * 8, tag);
        STAMP(5);
        __builtin_amdgcn_s_setprio(2);
        const int kh = w >> 1;
        int ln = lane;
        asm volatile("" : "+v"(ln));
        f32x16 o0, o1;
#pragma unroll
        for (int i = 0; i < 16; ++i) { o0[i] = 0.f; o1[i] = 0.f; }
        {
            unsigned d0 = Lb + 2048 * kh + (4 * (ln >> 5) + ((ln & 15) >> 2)) * 128 + (((2 * ((ln >> 4) & 1) + ((ln & 3) >> 1)) ^ (((ln & 15) >> 3) << 2)) << 4) + 8 * (ln & 1), d1 = d0 ^ 64;
            const unsigned hbeg = W_H + ((2 * tt + (w & 1)) * 86 + 43 * kh) * 1024;
            unsigned sho = hbeg + hrot * 1024, vf, vv, spin;
            const u64 stag = (u64)rfl(tg2.x) | ((u64)rfl(tg2.y) << 32);
            asm volatile("" : "+v"(o0), "+v"(o1));
            lwait(FL(L_WDF, 1), 4);
            asm volatile(
                "s_waitcnt lgkmcnt(0)\n\t"
                "ds_read_b64_tr_b16 v[96:97], %2 offset:0\n\t"
                "ds_read_b64_tr_b16 v[98:99], %2 offset:1024\n\t"
                "ds_read_b64_tr_b16 v[100:101], %3 offset:0\n\t"
                "ds_read_b64_tr_b16 v[102:103], %3 offset:1024\n\t"
                "s_cmp_lg_u32 %16, 0\n\t"
                "s_cbranch_scc1 Lg2_wait_%=\n\t"
                "s_mov_b32 %7, 0x40000\n\t"
                "Lg2_poll_%=:\n\t"
                "global_load_dwordx2 v[112:113], %12, off sc1\n\t"
                "s_sub_u32 %7, %7, 1\n\t"
                "s_cbranch_scc1 Lg2_seen_%=\n\t"
                "s_waitcnt vmcnt(0)\n\t"
                "v_cmp_ne_u64 vcc, %13, v[112:113]\n\t"
                "s_cbranch_vccz Lg2_seen_%=\n\t"
                "s_sleep 1\n\t"
                "s_branch Lg2_poll_%=\n\t"
                "Lg2_seen_%=:\n\t"
                "v_mov_b32 %6, 1\n\t"
                "s_mov_b64 exec, 1\n\t"
                "ds_write_b32 %17, %6\n\t"
                "s_mov_b64 exec, -1\n\t"
                "s_waitcnt vmcnt(0)\n\t"
                "Lg2_go_%=:\n\t"
                "s_waitcnt lgkmcnt(0)\n\t"
                "buffer_load_dwordx4 v[112:115], %8, %9, %4 offen sc1\n\t"
                "s_add_u32 %4, %4, 1024\n\t"
                "s_cmp_eq_u32 %4, %14\n\t"
                "s_cselect_b32 %4, %15, %4\n\t"
                "buffer_load_dwordx4 v[116:119], %8, %9, %4 offen sc1\n\t"
                "s_add_u32 %4, %4, 1024\n\t"
                "s_cmp_eq_u32 %4, %14\n\t"
                "s_cselect_b32 %4, %15, %4\n\t"
                "buffer_load_dwordx4 v[120:123], %8, %9, %4 offen sc1\n\t"
                "s_add_u32 %4, %4, 1024\n\t"
                "s_cmp_eq_u32 %4, %14\n\t"
                "s_cselect_b32 %4, %15, %4\n\t"
                "buffer_load_dwordx4 v[124:127], %8, %9, %4 offen sc1\n\t"
                "s_add_u32 %4, %4, 1024\n\t"
                "s_cmp_eq_u32 %4, %14\n\t"
                "s_cselect_b32 %4, %15, %4\n\t"
                "buffer_load_dwordx4 v[128:131], %8, %9, %4 offen sc1\n\t"
                "s_add_u32 %4, %4, 1024\n\t"
                "s_cmp_eq_u32 %4, %14\n\t"
                "s_cselect_b32 %4, %15, %4\n\t"
                "buffer_load_dwordx4 v[132:135], %8, %9, %4 offen sc1\n\t"
                "s_add_u32 %4, %4, 1024\n\t"
                "s_cmp_eq_u32 %4, %14\n\t"
                "s_cselect_b32 %4, %15, %4\n\t"
                "buffer_load_dwordx4 v[136:139], %8, %9, %4 offen sc1\n\t"
                "s_add_u32 %4, %4, 1024\n\t"
                "s_cmp_eq_u32 %4, %14\n\t"
                "s_cselect_b32 %4, %15, %4\n\t"
                "buffer_load_dwordx4 v[140:143], %8, %9, %4 offen sc1\n\t"
                "s_add_u32 %4, %4, 1024\n\t"
                "s_cmp_eq_u32 %4, %14\n\t"
                "s_cselect_b32 %4, %15, %4\n\t"
                "buffer_load_dwordx4 v[144:147], %8, %9, %4 offen sc1\n\t"
                "s_add_u32 %4, %4, 1024\n\t"
                "s_cmp_eq_u32 %4, %14\n\t"
                "s_cselect_b32 %4, %15, %4\n\t"
                "buffer_load_dwordx4 v[148:151], %8, %9, %4 offen sc1\n\t"
                "s_add_u32 %4, %4, 1024\n\t"
                "s_cmp_eq_u32 %4, %14\n\t"
                "s_cselect_b32 %4, %15, %4\n\t"
                "buffer_load_dwordx4 v[152:155], %8, %9, %4 offen sc1\n\t"
                "s_add_u32 %4, %4, 1024\n\t"
                "s_cmp_eq_u32 %4, %14\n\t"
                "s_cselect_b32 %4, %15, %4\n\t"
                "buffer_load_dwordx4 v[156:159], %8, %9, %4 offen sc1\n\t"
                "s_add_u32 %4, %4, 1024\n\t"
                "s_cmp_eq_u32 %4, %14\n\t"
                "s_cselect_b32 %4, %15, %4\n\t"
                "buffer_load_dwordx4 v[160:163], %8, %9, %4 offen sc1\n\t"
                "s_add_u32 %4, %4, 1024\n\t"
                "s_cmp_eq_u32 %4, %14\n\t"
                "s_cselect_b32 %4, %15, %4\n\t"
                "buffer_load_dwordx4 v[164:167], %8, %9, %4 offen sc1\n\t"
                "s_add_u32 %4, %4, 1024\n\t"
                "s_cmp_eq_u32 %4, %14\n\t"
                "s_cselect_b32 %4, %15, %4\n\t"
                "s_waitcnt vmcnt(13) lgkmcnt(2)\n\t"
                "v_mfma_f32_32x32x16_bf16 %0, v[96:99], v[112:115], %0\n\t"
                "ds_read_b64_tr_b16 v[104:105], %2 offset:4096\n\t"
                "ds_read_b64_tr_b16 v[106:107], %2 offset:5120\n\t"
                "s_waitcnt lgkmcnt(2)\n\t"
                "v_mfma_f32_32x32x16_bf16 %1, v[100:103], v[112:115], %1\n\t"
                "ds_read_b64_tr_b16 v[108:109], %3 offset:4096\n\t"
                "ds_read_b64_tr_b16 v[110:111], %3 offset:5120\n\t"
                "buffer_load_dwordx4 v[112:115], %8, %9, %4 offen sc1\n\t"
                "s_add_u32 %4, %4, 1024\n\t"
                "s_cmp_eq_u32 %4, %14\n\t"
                "s_cselect_b32 %4, %15, %4\n\t"
                "s_waitcnt vmcnt(13) lgkmcnt(2)\n\t"
                "v_mfma_f32_32x32x16_bf16 %0, v[104:107], v[116:119], %0\n\t"
                "ds_read_b64_tr_b16 v[96:97], %2 offset:8192\n\t"
                "ds_read_b64_tr_b16 v[98:99], %2 offset:9216\n\t"
                "s_waitcnt lgkmcnt(2)\n\t"
                "v_mfma_f32_32x32x16_bf16 %1, v[108:111], v[116:119], %1\n\t"
                "ds_read_b64_tr_b16 v[100:101], %3 offset:8192\n\t"
                "ds_read_b64_tr_b16 v[102:103], %3 offset:9216\n\t"
                "buffer_load_dwordx4 v[116:119], %8, %9, %4 offen sc1\n\t"
                "s_add_u32 %4, %4, 1024\n\t"
                "s_cmp_eq_u32 %4, %14\n\t"
                "s_cselect_b32 %4, %15, %4\n\t"
                "s_waitcnt vmcnt(13) lgkmcnt(2)\n\t"
                "v_mfma_f32_32x32x16_bf16 %0, v[96:99], v[120:123], %0\n\t"
                "ds_read_b64_tr_b16 v[104:105], %2 offset:12288\n\t"
                "ds_read_b64_tr_b16 v[106:107], %2 offset:13312\n\t"
                "s_waitcnt lgkmcnt(2)\n\t"
                "v_mfma_f32_32x32x16_bf16 %1, v[100:103], v[120:123], %1\n\t"
                "ds_read_b64_tr_b16 v[108:109], %3 offset:12288\n\t"
                "ds_read_b64_tr_b16 v[110:111], %3 offset:13312\n\t"
                "buffer_load_dwordx4 v[120:123], %8, %9, %4 offen sc1\n\t"
                "s_add_u32 %4, %4, 1024\n\t"
                "s_cmp_eq_u32 %4, %14\n\t"
                "s_cselect_b32 %4, %15, %4\n\t"
                "s_waitcnt vmcnt(13) lgkmcnt(2)\n\t"
                "v_mfma_f32_32x32x16_bf16 %0, v[104:107], v[124:127], %0\n\t"
                "ds_read_b64_tr_b16 v[96:97], %2 offset:16384\n\t"
                "ds_read_b64_tr_b16 v[98:99], %2 offset:17408\n\t"
                "s_waitcnt lgkmcnt(2)\n\t"
                "v_mfma_f32_32x32x16_bf16 %1, v[108:111], v[124:127], %1\n\t"
                "ds_read_b64_tr_b16 v[100:101], %3 offset:16384\n\t"
                "ds_read_b64_tr_b16 v[102:103], %3 offset:17408\n\t"
                "buffer_load_dwordx4 v[124:127], %8, %9, %4 offen sc1\n\t"
                "s_add_u32 %4, %4, 1024\n\t"
                "s_cmp_eq_u32 %4, %14\n\t"
                "s_cselect_b32 %4, %15, %4\n\t"
                "s_waitcnt vmcnt(13) lgkmcnt(2)\n\t"
                "v_mfma_f32_32x32x16_bf16 %0, v[96:99], v[128:131], %0\n\t"
                "ds_read_b64_tr_b16 v[104:105], %2 offset:20480\n\t"
                "ds_read_b64_tr_b16 v[106:107], %2 offset:21504\n\t"
                "s_waitcnt lgkmcnt(2)\n\t"
                "v_mfma_f32_32x32x16_bf16 %1, v[100:103], v[128:131], %1\n\t"
                "ds_read_b64_tr_b16 v[108:109], %3 offset:20480\n\t"
                "ds_read_b64_tr_b16 v[110:111], %3 offset:21504\n\t"
                "v_mov_b32 %6, 10\n\t"
                "ds_write_b32 %10, %6\n\t"
                "buffer_load_dwordx4 v[128:131], %8, %9, %4 offen sc1\n\t"
                "s_add_u32 %4, %4, 1024\n\t"
                "s_cmp_eq_u32 %4, %14\n\t"
                "s_cselect_b32 %4, %15, %4\n\t"
                "s_waitcnt vmcnt(13) lgkmcnt(3)\n\t"
                "v_mfma_f32_32x32x16_bf16 %0, v[104:107], v[132:135], %0\n\t"
                "ds_read_b64_tr_b16 v[96:97], %2 offset:24576\n\t"
                "ds_read_b64_tr_b16 v[98:99], %2 offset:25600\n\t"
                "s_waitcnt lgkmcnt(2)\n\t"
                "v_mfma_f32_32x32x16_bf16 %1, v[108:111], v[132:135], %1\n\t"
                "ds_read_b64_tr_b16 v[100:101], %3 offset:24576\n\t"
                "ds_read_b64_tr_b16 v[102:103], %3 offset:25600\n\t"
                "buffer_load_dwordx4 v[132:135], %8, %9, %4 offen sc1\n\t"
                "s_add_u32 %4, %4, 1024\n\t"
                "s_cmp_eq_u32 %4, %14\n\t"
                "s_cselect_b32 %4, %15, %4\n\t"
                "s_waitcnt vmcnt(13) lgkmcnt(2)\n\t"
                "v_mfma_f32_32x32x16_bf16 %0, v[96:99], v[136:139], %0\n\t"
                "ds_read_b64_tr_b16 v[104:105], %2 offset:28672\n\t"
                "ds_read_b64_tr_b16 v[106:107], %2 offset:29696\n\t"
                "s_waitcnt lgkmcnt(2)\n\t"
                "v_mfma_f32_32x32x16_bf16 %1, v[100:103], v[136:139], %1\n\t"
                "ds_read_b64_tr_b16 v[108:109], %3 offset:28672\n\t"
                "ds_read_b64_tr_b16 v[110:111], %3 offset:29696\n\t"
                "buffer_load_dwordx4 v[136:139], %8, %9, %4 offen sc1\n\t"
                "s_add_u32 %4, %4, 1024\n\t"
                "s_cmp_eq_u32 %4, %14\n\t"
                "s_cselect_b32 %4, %15, %4\n\t"
                "s_waitcnt vmcnt(13) lgkmcnt(2)\n\t"
                "v_mfma_f32_32x32x16_bf16 %0, v[104:107], v[140:143], %0\n\t"
                "ds_read_b64_tr_b16 v[96:97], %2 offset:32768\n\t"
                "ds_read_b64_tr_b16 v[98:99], %2 offset:33792\n\t"
                "s_waitcnt lgkmcnt(2)\n\t"
                "v_mfma_f32_32x32x16_bf16 %1, v[108:111], v[140:143], %1\n\t"
                "ds_read_b64_tr_b16 v[100:101], %3 offset:32768\n\t"
                "ds_read_b64_tr_b16 v[102:103], %3 offset:33792\n\t"
                "buffer_load_dwordx4 v[140:143], %8, %9, %4 offen sc1\n\t"
                "s_add_u32 %4, %4, 1024\n\t"
                "s_cmp_eq_u32 %4, %14\n\t"
                "s_cselect_b32 %4, %15, %4\n\t"
                "s_waitcnt vmcnt(13) lgkmcnt(2)\n\t"
                "v_mfma_f32_32x32x16_bf16 %0, v[96:99], v[144:147], %0\n\t"
                "ds_read_b64_tr_b16 v[104:105], %2 offset:36864\n\t"
                "ds_read_b64_tr_b16 v[106:107], %2 offset:37888\n\t"
                "s_waitcnt lgkmcnt(2)\n\t"
                "v_mfma_f32_32x32x16_bf16 %1, v[100:103], v[144:147], %1\n\t"
                "ds_read_b64_tr_b16 v[108:109], %3 offset:36864\n\t"
                "ds_read_b64_tr_b16 v[110:111], %3 offset:37888\n\t"
                "buffer_load_dwordx4 v[144:147], %8, %9, %4 offen sc1\n\t"
                "s_add_u32 %4, %4, 1024\n\t"
                "s_cmp_eq_u32 %4, %14\n\t"
                "s_cselect_b32 %4, %15, %4\n\t"
                "s_waitcnt vmcnt(13) lgkmcnt(2)\n\t"
                "v_mfma_f32_32x32x16_bf16 %0, v[104:107], v[148:151], %0\n\t"
                "ds_read_b64_tr_b16 v[96:97], %2 offset:40960\n\t"
                "ds_read_b64_tr_b16 v[98:99], %2 offset:41984\n\t"
                "s_waitcnt lgkmcnt(2)\n\t"
                "v_mfma_f32_32x32x16_bf16 %1, v[108:111], v[148:151], %1\n\t"
                "ds_read_b64_tr_b16 v[100:101], %3 offset:40960\n\t"
                "ds_read_b64_tr_b16 v[102:103], %3 offset:41984\n\t"
                "buffer_load_dwordx4 v[148:151], %8, %9, %4 offen sc1\n\t"
                "s_add_u32 %4, %4, 1024\n\t"
                "s_cmp_eq_u32 %4, %14\n\t"
                "s_cselect_b32 %4, %15, %4\n\t"
                "s_waitcnt vmcnt(13) lgkmcnt(2)\n\t"
                "v_mfma_f32_32x32x16_bf16 %0, v[96:99], v[152:155], %0\n\t"
                "ds_read_b64_tr_b16 v[104:105], %2 offset:45056\n\t"
                "ds_read_b64_tr_b16 v[106:107], %2 offset:46080\n\t"
                "s_waitcnt lgkmcnt(2)\n\t"
                "v_mfma_f32_32x32x16_bf16 %1, v[100:103], v[152:155], %1\n\t"
                "ds_read_b64_tr_b16 v[108:109], %3 offset:45056\n\t"
                "ds_read_b64_tr_b16 v[110:111], %3 offset:46080\n\t"
                "buffer_load_dwordx4 v[152:155], %8, %9, %4 offen sc1\n\t"
                "s_add_u32 %4, %4, 1024\n\t"
                "s_cmp_eq_u32 %4, %14\n\t"
                "s_cselect_b32 %4, %15, %4\n\t"
                "s_waitcnt vmcnt(13) lgkmcnt(2)\n\t"
                "v_mfma_f32_32x32x16_bf16 %0, v[104:107], v[156:159], %0\n\t"
                "ds_read_b64_tr_b16 v[96:97], %2 offset:49152\n\t"
                "ds_read_b64_tr_b16 v[98:99], %2 offset:50176\n\t"
                "s_waitcnt lgkmcnt(2)\n\t"
                "v_mfma_f32_32x32x16_bf16 %1, v[108:111], v[156:159], %1\n\t"
                "ds_read_b64_tr_b16 v[100:101], %3 offset:49152\n\t"
                "ds_read_b64_tr_b16 v[102:103], %3 offset:50176\n\t"
                "buffer_load_dwordx4 v[156:159], %8, %9, %4 offen sc1\n\t"
                "s_add_u32 %4, %4, 1024\n\t"
                "s_cmp_eq_u32 %4, %14\n\t"
                "s_cselect_b32 %4, %15, %4\n\t"
                "s_waitcnt vmcnt(13) lgkmcnt(2)\n\t"
                "v_mfma_f32_32x32x16_bf16 %0, v[96:99], v[160:163], %0\n\t"
                "ds_read_b64_tr_b16 v[104:105], %2 offset:53248\n\t"
                "ds_read_b64_tr_b16 v[106:107], %2 offset:54272\n\t"
                "s_waitcnt lgkmcnt(2)\n\t"
                "v_mfma_f32_32x32x16_bf16 %1, v[100:103], v[160:163], %1\n\t"
                "ds_read_b64_tr_b16 v[108:109], %3 offset:53248\n\t"
                "ds_read_b64_tr_b16 v[110:111], %3 offset:54272\n\t"
                "buffer_load_dwordx4 v[160:163], %8, %9, %4 offen sc1\n\t"
                "s_add_u32 %4, %4, 1024\n\t"
                "s_cmp_eq_u32 %4, %14\n\t"
                "s_cselect_b32 %4, %15, %4\n\t"
                "s_waitcnt vmcnt(13) lgkmcnt(2)\n\t"
                "v_mfma_f32_32x32x16_bf16 %0, v[104:107], v[164:167], %0\n\t"
                "ds_read_b64_tr_b16 v[96:97], %2 offset:57344\n\t"
                "ds_read_b64_tr_b16 v[98:99], %2 offset:58368\n\t"
                "s_waitcnt lgkmcnt(2)\n\t"
                "v_mfma_f32_32x32x16_bf16 %1, v[108:111], v[164:167], %1\n\t"
                "ds_read_b64_tr_b16 v[100:101], %3 offset:57344\n\t"
                "ds_read_b64_tr_b16 v[102:103], %3 offset:58368\n\t"
                "v_mov_b32 %6, 11\n\t"
                "ds_write_b32 %10, %6\n\t"
                "buffer_load_dwordx4 v[164:167], %8, %9, %4 offen sc1\n\t"
                "s_add_u32 %4, %4, 1024\n\t"
                "s_cmp_eq_u32 %4, %14\n\t"
                "s_cselect_b32 %4, %15, %4\n\t"
                "s_waitcnt vmcnt(13) lgkmcnt(3)\n\t"
                "v_mfma_f32_32x32x16_bf16 %0, v[96:99], v[112:115], %0\n\t"
                "ds_read_b64_tr_b16 v[104:105], %2 offset:61440\n\t"
                "ds_read_b64_tr_b16 v[106:107], %2 offset:62464\n\t"
                "s_waitcnt lgkmcnt(2)\n\t"
                "v_mfma_f32_32x32x16_bf16 %1, v[100:103], v[112:115], %1\n\t"
                "ds_read_b64_tr_b16 v[108:109], %3 offset:61440\n\t"
                "ds_read_b64_tr_b16 v[110:111], %3 offset:62464\n\t"
                "buffer_load_dwordx4 v[112:115], %8, %9, %4 offen sc1\n\t"
                "s_add_u32 %4, %4, 1024\n\t"
                "s_cmp_eq_u32 %4, %14\n\t"
                "s_cselect_b32 %4, %15, %4\n\t"
                "v_add_u32 %2, 65536, %2\n\t"
                "v_add_u32 %3, 65536, %3\n\t"
                "s_waitcnt vmcnt(13) lgkmcnt(2)\n\t"
                "v_mfma_f32_32x32x16_bf16 %0, v[104:107], v[116:119], %0\n\t"
                "ds_read_b64_tr_b16 v[96:97], %2 offset:0\n\t"
                "ds_read_b64_tr_b16 v[98:99], %2 offset:1024\n\t"
                "s_waitcnt lgkmcnt(2)\n\t"
                "v_mfma_f32_32x32x16_bf16 %1, v[108:111], v[116:119], %1\n\t"
                "ds_read_b64_tr_b16 v[100:101], %3 offset:0\n\t"
                "ds_read_b64_tr_b16 v[102:103], %3 offset:1024\n\t"
                "buffer_load_dwordx4 v[116:119], %8, %9, %4 offen sc1\n\t"
                "s_add_u32 %4, %4, 1024\n\t"
                "s_cmp_eq_u32 %4, %14\n\t"
                "s_cselect_b32 %4, %15, %4\n\t"
                "s_waitcnt vmcnt(13) lgkmcnt(2)\n\t"
                "v_mfma_f32_32x32x16_bf16 %0, v[96:99], v[120:123], %0\n\t"
                "ds_read_b64_tr_b16 v[104:105], %2 offset:4096\n\t"
                "ds_read_b64_tr_b16 v[106:107], %2 offset:5120\n\t"
                "s_waitcnt lgkmcnt(2)\n\t"
                "v_mfma_f32_32x32x16_bf16 %1, v[100:103], v[120:123], %1\n\t"
                "ds_read_b64_tr_b16 v[108:109], %3 offset:4096\n\t"
                "ds_read_b64_tr_b16 v[110:111], %3 offset:5120\n\t"
                "buffer_load_dwordx4 v[120:123], %8, %9, %4 offen sc1\n\t"
                "s_add_u32 %4, %4, 1024\n\t"
                "s_cmp_eq_u32 %4, %14\n\t"
                "s_cselect_b32 %4, %15, %4\n\t"
                "s_waitcnt vmcnt(13) lgkmcnt(2)\n\t"
                "v_mfma_f32_32x32x16_bf16 %0, v[104:107], v[124:127], %0\n\t"
                "ds_read_b64_tr_b16 v[96:97], %2 offset:8192\n\t"
                "ds_read_b64_tr_b16 v[98:99], %2 offset:9216\n\t"
                "s_waitcnt lgkmcnt(2)\n\t"
                "v_mfma_f32_32x32x16_bf16 %1, v[108:111], v[124:127], %1\n\t"
                "ds_read_b64_tr_b16 v[100:101], %3 offset:8192\n\t"
                "ds_read_b64_tr_b16 v[102:103], %3 offset:9216\n\t"
                "buffer_load_dwordx4 v[124:127], %8, %9, %4 offen sc1\n\t"
                "s_add_u32 %4, %4, 1024\n\t"
                "s_cmp_eq_u32 %4, %14\n\t"
                "s_cselect_b32 %4, %15, %4\n\t"
                "s_waitcnt vmcnt(13) lgkmcnt(2)\n\t"
                "v_mfma_f32_32x32x16_bf16 %0, v[96:99], v[128:131], %0\n\t"
                "ds_read_b64_tr_b16 v[104:105], %2 offset:12288\n\t"
                "ds_read_b64_tr_b16 v[106:107], %2 offset:13312\n\t"
                "s_waitcnt lgkmcnt(2)\n\t"
                "v_mfma_f32_32x32x16_bf16 %1, v[100:103], v[128:131], %1\n\t"
                "ds_read_b64_tr_b16 v[108:109], %3 offset:12288\n\t"
                "ds_read_b64_tr_b16 v[110:111], %3 offset:13312\n\t"
                "buffer_load_dwordx4 v[128:131], %8, %9, %4 offen sc1\n\t"
                "s_add_u32 %4, %4, 1024\n\t"
                "s_cmp_eq_u32 %4, %14\n\t"
                "s_cselect_b32 %4, %15, %4\n\t"
                "s_waitcnt vmcnt(13) lgkmcnt(2)\n\t"
                "v_mfma_f32_32x32x16_bf16 %0, v[104:107], v[132:135], %0\n\t"
                "ds_read_b64_tr_b16 v[96:97], %2 offset:16384\n\t"
                "ds_read_b64_tr_b16 v[98:99], %2 offset:17408\n\t"
                "s_waitcnt lgkmcnt(2)\n\t"
                "v_mfma_f32_32x32x16_bf16 %1, v[108:111], v[132:135], %1\n\t"
                "ds_read_b64_tr_b16 v[100:101], %3 offset:16384\n\t"
                "ds_read_b64_tr_b16 v[102:103], %3 offset:17408\n\t"
                "buffer_load_dwordx4 v[132:135], %8, %9, %4 offen sc1\n\t"
                "s_add_u32 %4, %4, 1024\n\t"
                "s_cmp_eq_u32 %4, %14\n\t"
                "s_cselect_b32 %4, %15, %4\n\t"
                "s_waitcnt vmcnt(13) lgkmcnt(2)\n\t"
                "v_mfma_f32_32x32x16_bf16 %0, v[96:99], v[136:139], %0\n\t"
                "ds_read_b64_tr_b16 v[104:105], %2 offset:20480\n\t"
                "ds_read_b64_tr_b16 v[106:107], %2 offset:21504\n\t"
                "s_waitcnt lgkmcnt(2)\n\t"
                "v_mfma_f32_32x32x16_bf16 %1, v[100:103], v[136:139], %1\n\t"
                "ds_read_b32 %5, %11\n\t"
                "ds_read_b64_tr_b16 v[108:109], %3 offset:20480\n\t"
                "ds_read_b64_tr_b16 v[110:111], %3 offset:21504\n\t"
                "buffer_load_dwordx4 v[136:139], %8, %9, %4 offen sc1\n\t"
                "s_add_u32 %4, %4, 1024\n\t"
                "s_cmp_eq_u32 %4, %14\n\t"
                "s_cselect_b32 %4, %15, %4\n\t"
                "s_waitcnt vmcnt(13) lgkmcnt(2)\n\t"
                "v_mfma_f32_32x32x16_bf16 %0, v[104:107], v[140:143], %0\n\t"
                "ds_read_b64_tr_b16 v[96:97], %2 offset:24576\n\t"
                "ds_read_b64_tr_b16 v[98:99], %2 offset:25600\n\t"
                "s_waitcnt lgkmcnt(2)\n\t"
                "v_mfma_f32_32x32x16_bf16 %1, v[108:111], v[140:143], %1\n\t"
                "ds_read_b64_tr_b16 v[100:101], %3 offset:24576\n\t"
                "ds_read_b64_tr_b16 v[102:103], %3 offset:25600\n\t"
                "buffer_load_dwordx4 v[140:143], %8, %9, %4 offen sc1\n\t"
                "s_add_u32 %4, %4, 1024\n\t"
                "s_cmp_eq_u32 %4, %14\n\t"
                "s_cselect_b32 %4, %15, %4\n\t"
                "v_cmp_gt_u32 vcc, 1, %5\n\t"
                "s_cbranch_vccnz Lg2_slow1_%=\n\t"
                "Lg2_back1_%=:\n\t"
                "s_waitcnt vmcnt(13) lgkmcnt(2)\n\t"
                "v_mfma_f32_32x32x16_bf16 %0, v[96:99], v[144:147], %0\n\t"
                "ds_read_b64_tr_b16 v[104:105], %2 offset:28672\n\t"
                "ds_read_b64_tr_b16 v[106:107], %2 offset:29696\n\t"
                "s_waitcnt lgkmcnt(2)\n\t"
                "v_mfma_f32_32x32x16_bf16 %1, v[100:103], v[144:147], %1\n\t"
                "ds_read_b64_tr_b16 v[108:109], %3 offset:28672\n\t"
                "ds_read_b64_tr_b16 v[110:111], %3 offset:29696\n\t"
                "buffer_load_dwordx4 v[144:147], %8, %9, %4 offen sc1\n\t"
                "s_add_u32 %4, %4, 1024\n\t"
                "s_cmp_eq_u32 %4, %14\n\t"
                "s_cselect_b32 %4, %15, %4\n\t"
                "s_waitcnt vmcnt(13) lgkmcnt(2)\n\t"
                "v_mfma_f32_32x32x16_bf16 %0, v[104:107], v[148:151], %0\n\t"
                "ds_read_b64_tr_b16 v[96:97], %2 offset:32768\n\t"
                "ds_read_b64_tr_b16 v[98:99], %2 offset:33792\n\t"
                "s_waitcnt lgkmcnt(2)\n\t"
                "v_mfma_f32_32x32x16_bf16 %1, v[108:111], v[148:151], %1\n\t"
                "ds_read_b64_tr_b16 v[100:101], %3 offset:32768\n\t"
                "ds_read_b64_tr_b16 v[102:103], %3 offset:33792\n\t"
                "buffer_load_dwordx4 v[148:151], %8, %9, %4 offen sc1\n\t"
                "s_add_u32 %4, %4, 1024\n\t"
                "s_cmp_eq_u32 %4, %14\n\t"
                "s_cselect_b32 %4, %15, %4\n\t"
                "s_waitcnt vmcnt(13) lgkmcnt(2)\n\t"
                "v_mfma_f32_32x32x16_bf16 %0, v[96:99], v[152:155], %0\n\t"
                "ds_read_b64_tr_b16 v[104:105], %2 offset:36864\n\t"
                "ds_read_b64_tr_b16 v[106:107], %2 offset:37888\n\t"
                "s_waitcnt lgkmcnt(2)\n\t"
                "v_mfma_f32_32x32x16_bf16 %1, v[100:103], v[152:155], %1\n\t"
                "ds_read_b64_tr_b16 v[108:109], %3 offset:36864\n\t"
                "ds_read_b64_tr_b16 v[110:111], %3 offset:37888\n\t"
                "buffer_load_dwordx4 v[152:155], %8, %9, %4 offen sc1\n\t"
                "s_add_u32 %4, %4, 1024\n\t"
                "s_cmp_eq_u32 %4, %14\n\t"
                "s_cselect_b32 %4, %15, %4\n\t"
                "s_waitcnt vmcnt(13) lgkmcnt(2)\n\t"
                "v_mfma_f32_32x32x16_bf16 %0, v[104:107], v[156:159], %0\n\t"
                "ds_read_b64_tr_b16 v[96:97], %2 offset:40960\n\t"
                "ds_read_b64_tr_b16 v[98:99], %2 offset:41984\n\t"
                "s_waitcnt lgkmcnt(2)\n\t"
                "v_mfma_f32_32x32x16_bf16 %1, v[108:111], v[156:159], %1\n\t"
                "ds_read_b64_tr_b16 v[100:101], %3 offset:40960\n\t"
                "ds_read_b64_tr_b16 v[102:103], %3 offset:41984\n\t"
                "buffer_load_dwordx4 v[156:159], %8, %9, %4 offen sc1\n\t"
                "s_add_u32 %4, %4, 1024\n\t"
                "s_cmp_eq_u32 %4, %14\n\t"
                "s_cselect_b32 %4, %15, %4\n\t"
                "s_waitcnt vmcnt(13) lgkmcnt(2)\n\t"
                "v_mfma_f32_32x32x16_bf16 %0, v[96:99], v[160:163], %0\n\t"
                "ds_read_b64_tr_b16 v[104:105], %2 offset:45056\n\t"
                "ds_read_b64_tr_b16 v[106:107], %2 offset:46080\n\t"
                "s_waitcnt lgkmcnt(2)\n\t"
                "v_mfma_f32_32x32x16_bf16 %1, v[100:103], v[160:163], %1\n\t"
                "ds_read_b64_tr_b16 v[108:109], %3 offset:45056\n\t"
                "ds_read_b64_tr_b16 v[110:111], %3 offset:46080\n\t"
                "buffer_load_dwordx4 v[160:163], %8, %9, %4 offen sc1\n\t"
                "s_add_u32 %4, %4, 1024\n\t"
                "s_cmp_eq_u32 %4, %14\n\t"
                "s_cselect_b32 %4, %15, %4\n\t"
                "s_waitcnt vmcnt(13) lgkmcnt(2)\n\t"
                "v_mfma_f32_32x32x16_bf16 %0, v[104:107], v[164:167], %0\n\t"
                "ds_read_b64_tr_b16 v[96:97], %2 offset:49152\n\t"
                "ds_read_b64_tr_b16 v[98:99], %2 offset:50176\n\t"
                "s_waitcnt lgkmcnt(2)\n\t"
                "v_mfma_f32_32x32x16_bf16 %1, v[108:111], v[164:167], %1\n\t"
                "ds_read_b64_tr_b16 v[100:101], %3 offset:49152\n\t"
                "ds_read_b64_tr_b16 v[102:103], %3 offset:50176\n\t"
                "buffer_load_dwordx4 v[164:167], %8, %9, %4 offen sc1\n\t"
                "s_add_u32 %4, %4, 1024\n\t"
                "s_cmp_eq_u32 %4, %14\n\t"
                "s_cselect_b32 %4, %15, %4\n\t"
                "s_waitcnt vmcnt(13) lgkmcnt(2)\n\t"
                "v_mfma_f32_32x32x16_bf16 %0, v[96:99], v[112:115], %0\n\t"
                "ds_read_b64_tr_b16 v[104:105], %2 offset:53248\n\t"
                "ds_read_b64_tr_b16 v[106:107], %2 offset:54272\n\t"
                "s_waitcnt lgkmcnt(2)\n\t"
                "v_mfma_f32_32x32x16_bf16 %1, v[100:103], v[112:115], %1\n\t"
                "ds_read_b64_tr_b16 v[108:109], %3 offset:53248\n\t"
                "ds_read_b64_tr_b16 v[110:111], %3 offset:54272\n\t"
                "buffer_load_dwordx4 v[112:115], %8, %9, %4 offen sc1\n\t"
                "s_add_u32 %4, %4, 1024\n\t"
                "s_cmp_eq_u32 %4, %14\n\t"
                "s_cselect_b32 %4, %15, %4\n\t"
                "s_waitcnt vmcnt(13) lgkmcnt(2)\n\t"
                "v_mfma_f32_32x32x16_bf16 %0, v[104:107], v[116:119], %0\n\t"
                "ds_read_b64_tr_b16 v[96:97], %2 offset:57344\n\t"
                "ds_read_b64_tr_b16 v[98:99], %2 offset:58368\n\t"
                "s_waitcnt lgkmcnt(2)\n\t"
                "v_mfma_f32_32x32x16_bf16 %1, v[108:111], v[116:119], %1\n\t"
                "ds_read_b64_tr_b16 v[100:101], %3 offset:57344\n\t"
                "ds_read_b64_tr_b16 v[102:103], %3 offset:58368\n\t"
                "s_waitcnt vmcnt(12) lgkmcnt(2)\n\t"
                "v_mfma_f32_32x32x16_bf16 %0, v[96:99], v[120:123], %0\n\t"
                "ds_read_b64_tr_b16 v[104:105], %2 offset:61440\n\t"
                "ds_read_b64_tr_b16 v[106:107], %2 offset:62464\n\t"
                "s_waitcnt lgkmcnt(2)\n\t"
                "v_mfma_f32_32x32x16_bf16 %1, v[100:103], v[120:123], %1\n\t"
                "ds_read_b64_tr_b16 v[108:109], %3 offset:61440\n\t"
                "ds_read_b64_tr_b16 v[110:111], %3 offset:62464\n\t"
                "v_add_u32 %2, 65536, %2\n\t"
                "v_add_u32 %3, 65536, %3\n\t"
                "s_waitcnt vmcnt(11) lgkmcnt(2)\n\t"
                "v_mfma_f32_32x32x16_bf16 %0, v[104:107], v[124:127], %0\n\t"
                "ds_read_b64_tr_b16 v[96:97], %2 offset:0\n\t"
                "ds_read_b64_tr_b16 v[98:99], %2 offset:1024\n\t"
                "s_waitcnt lgkmcnt(2)\n\t"
                "v_mfma_f32_32x32x16_bf16 %1, v[108:111], v[124:127], %1\n\t"
                "ds_read_b64_tr_b16 v[100:101], %3 offset:0\n\t"
                "ds_read_b64_tr_b16 v[102:103], %3 offset:1024\n\t"
                "s_waitcnt vmcnt(10) lgkmcnt(2)\n\t"
                "v_mfma_f32_32x32x16_bf16 %0, v[96:99], v[128:131], %0\n\t"
                "ds_read_b64_tr_b16 v[104:105], %2 offset:4096\n\t"
                "ds_read_b64_tr_b16 v[106:107], %2 offset:5120\n\t"
                "s_waitcnt lgkmcnt(2)\n\t"
                "v_mfma_f32_32x32x16_bf16 %1, v[100:103], v[128:131], %1\n\t"
                "ds_read_b64_tr_b16 v[108:109], %3 offset:4096\n\t"
                "ds_read_b64_tr_b16 v[110:111], %3 offset:5120\n\t"
                "s_waitcnt vmcnt(9) lgkmcnt(2)\n\t"
                "v_mfma_f32_32x32x16_bf16 %0, v[104:107], v[132:135], %0\n\t"
                "ds_read_b64_tr_b16 v[96:97], %2 offset:8192\n\t"
                "ds_read_b64_tr_b16 v[98:99], %2 offset:9216\n\t"
                "s_waitcnt lgkmcnt(2)\n\t"
                "v_mfma_f32_32x32x16_bf16 %1, v[108:111], v[132:135], %1\n\t"
                "ds_read_b64_tr_b16 v[100:101], %3 offset:8192\n\t"
                "ds_read_b64_tr_b16 v[102:103], %3 offset:9216\n\t"
                "s_waitcnt vmcnt(8) lgkmcnt(2)\n\t"
                "v_mfma_f32_32x32x16_bf16 %0, v[96:99], v[136:139], %0\n\t"
                "ds_read_b64_tr_b16 v[104:105], %2 offset:12288\n\t"
                "ds_read_b64_tr_b16 v[106:107], %2 offset:13312\n\t"
                "s_waitcnt lgkmcnt(2)\n\t"
                "v_mfma_f32_32x32x16_bf16 %1, v[100:103], v[136:139], %1\n\t"
                "ds_read_b32 %5, %11\n\t"
                "ds_read_b64_tr_b16 v[108:109], %3 offset:12288\n\t"
                "ds_read_b64_tr_b16 v[110:111], %3 offset:13312\n\t"
                "s_waitcnt vmcnt(7) lgkmcnt(2)\n\t"
                "v_mfma_f32_32x32x16_bf16 %0, v[104:107], v[140:143], %0\n\t"
                "ds_read_b64_tr_b16 v[96:97], %2 offset:16384\n\t"
                "ds_read_b64_tr_b16 v[98:99], %2 offset:17408\n\t"
                "s_waitcnt lgkmcnt(2)\n\t"
                "v_mfma_f32_32x32x16_bf16 %1, v[108:111], v[140:143], %1\n\t"
                "ds_read_b64_tr_b16 v[100:101], %3 offset:16384\n\t"
                "ds_read_b64_tr_b16 v[102:103], %3 offset:17408\n\t"
                "v_cmp_gt_u32 vcc, 2, %5\n\t"
                "s_cbranch_vccnz Lg2_slow2_%=\n\t"
                "Lg2_back2_%=:\n\t"
                "v_add_u32 %2, -131072, %2\n\t"
                "v_add_u32 %3, -131072, %3\n\t"
                "s_waitcnt vmcnt(6) lgkmcnt(2)\n\t"
                "v_mfma_f32_32x32x16_bf16 %0, v[96:99], v[144:147], %0\n\t"
                "ds_read_b64_tr_b16 v[104:105], %2 offset:0\n\t"
                "ds_read_b64_tr_b16 v[106:107], %2 offset:1024\n\t"
                "s_waitcnt lgkmcnt(2)\n\t"
                "v_mfma_f32_32x32x16_bf16 %1, v[100:103], v[144:147], %1\n\t"
                "ds_read_b64_tr_b16 v[108:109], %3 offset:0\n\t"
                "ds_read_b64_tr_b16 v[110:111], %3 offset:1024\n\t"
                "s_waitcnt vmcnt(5) lgkmcnt(2)\n\t"
                "v_mfma_f32_32x32x16_bf16 %0, v[104:107], v[148:151], %0\n\t"
                "ds_read_b64_tr_b16 v[96:97], %2 offset:4096\n\t"
                "ds_read_b64_tr_b16 v[98:99], %2 offset:5120\n\t"
                "s_waitcnt lgkmcnt(2)\n\t"
                "v_mfma_f32_32x32x16_bf16 %1, v[108:111], v[148:151], %1\n\t"
                "ds_read_b64_tr_b16 v[100:101], %3 offset:4096\n\t"
                "ds_read_b64_tr_b16 v[102:103], %3 offset:5120\n\t"
                "s_waitcnt vmcnt(4) lgkmcnt(2)\n\t"
                "v_mfma_f32_32x32x16_bf16 %0, v[96:99], v[152:155], %0\n\t"
                "ds_read_b64_tr_b16 v[104:105], %2 offset:8192\n\t"
                "ds_read_b64_tr_b16 v[106:107], %2 offset:9216\n\t"
                "s_waitcnt lgkmcnt(2)\n\t"
                "v_mfma_f32_32x32x16_bf16 %1, v[100:103], v[152:155], %1\n\t"
                "ds_read_b64_tr_b16 v[108:109], %3 offset:8192\n\t"
                "ds_read_b64_tr_b16 v[110:111], %3 offset:9216\n\t"
                "s_waitcnt vmcnt(3) lgkmcnt(2)\n\t"
                "v_mfma_f32_32x32x16_bf16 %0, v[104:107], v[156:159], %0\n\t"
                "ds_read_b64_tr_b16 v[96:97], %2 offset:12288\n\t"
                "ds_read_b64_tr_b16 v[98:99], %2 offset:13312\n\t"
                "s_waitcnt lgkmcnt(2)\n\t"
                "v_mfma_f32_32x32x16_bf16 %1, v[108:111], v[156:159], %1\n\t"
                "ds_read_b64_tr_b16 v[100:101], %3 offset:12288\n\t"
                "ds_read_b64_tr_b16 v[102:103], %3 offset:13312\n\t"
                "s_waitcnt vmcnt(2) lgkmcnt(2)\n\t"
                "v_mfma_f32_32x32x16_bf16 %0, v[96:99], v[160:163], %0\n\t"
                "ds_read_b64_tr_b16 v[104:105], %2 offset:16384\n\t"
                "ds_read_b64_tr_b16 v[106:107], %2 offset:17408\n\t"
                "s_waitcnt lgkmcnt(2)\n\t"
                "v_mfma_f32_32x32x16_bf16 %1, v[100:103], v[160:163], %1\n\t"
                "ds_read_b64_tr_b16 v[108:109], %3 offset:16384\n\t"
                "ds_read_b64_tr_b16 v[110:111], %3 offset:17408\n\t"
                "s_waitcnt vmcnt(1) lgkmcnt(2)\n\t"
                "v_mfma_f32_32x32x16_bf16 %0, v[104:107], v[164:167], %0\n\t"
                "ds_read_b64_tr_b16 v[96:97], %2 offset:20480\n\t"
                "ds_read_b64_tr_b16 v[98:99], %2 offset:21504\n\t"
                "s_waitcnt lgkmcnt(2)\n\t"
                "v_mfma_f32_32x32x16_bf16 %1, v[108:111], v[164:167], %1\n\t"
                "ds_read_b64_tr_b16 v[100:101], %3 offset:20480\n\t"
                "ds_read_b64_tr_b16 v[102:103], %3 offset:21504\n\t"
                "s_waitcnt vmcnt(0) lgkmcnt(2)\n\t"
                "v_mfma_f32_32x32x16_bf16 %0, v[96:99], v[112:115], %0\n\t"
                "s_waitcnt lgkmcnt(0)\n\t"
                "v_mfma_f32_32x32x16_bf16 %1, v[100:103], v[112:115], %1\n\t"
                "s_branch Lg2_done_%=\n\t"
                "Lg2_wait_%=:\n\t"
                "s_mov_b32 %7, 0x400000\n\t"
                "Lg2_waits_%=:\n\t"
                "s_sleep 1\n\t"
                "ds_read_b32 %5, %17\n\t"
                "s_sub_u32 %7, %7, 1\n\t"
                "s_cbranch_scc1 Lg2_go_%=\n\t"
                "s_waitcnt lgkmcnt(0)\n\t"
                "v_cmp_gt_u32 vcc, 1, %5\n\t"
                "s_cbranch_vccnz Lg2_waits_%=\n\t"
                "s_branch Lg2_go_%=\n\t"
                "Lg2_slow1_%=:\n\t"
                "s_mov_b32 %7, 0x400000\n\t"
                "Lg2_slow1s_%=:\n\t"
                "ds_read_b32 %5, %11\n\t"
                "s_sub_u32 %7, %7, 1\n\t"
                "s_cbranch_scc1 Lg2_back1_%=\n\t"
                "s_waitcnt lgkmcnt(0)\n\t"
                "v_cmp_gt_u32 vcc, 1, %5\n\t"
                "s_cbranch_vccnz Lg2_slow1s_%=\n\t"
                "s_branch Lg2_back1_%=\n\t"
                "Lg2_slow2_%=:\n\t"
                "s_mov_b32 %7, 0x400000\n\t"
                "Lg2_slow2s_%=:\n\t"
                "ds_read_b32 %5, %11\n\t"
                "s_sub_u32 %7, %7, 1\n\t"
                "s_cbranch_scc1 Lg2_back2_%=\n\t"
                "s_waitcnt lgkmcnt(0)\n\t"
                "v_cmp_gt_u32 vcc, 2, %5\n\t"
                "s_cbranch_vccnz Lg2_slow2s_%=\n\t"
                "s_branch Lg2_back2_%=\n\t"
                "Lg2_done_%=:\n\t"
                "s_waitcnt lgkmcnt(0)\n\t"
                "s_nop 15\n\t"
                : "+v"(o0), "+v"(o1), "+v"(d0), "+v"(d1), "+s"(sho), "=&v"(vf), "=&v"(vv), "=&s"(spin)
                : "v"(ln * 16), "s"(mksrd(R, W_BYTES)), "v"(Lb + FRP + 256 * w + 4 * ln), "v"(Lb + FLG + 4 * (L_WDC + (ln & 3))),
                  "v"(R + W_INH + ((tt * 8 + ct) * 32 + (ln & 31)) * 8), "s"(stag), "s"(hbeg + 43 * 1024), "s"(hbeg), "s"(w), "v"(Lb + FLG + 4 * L_HGO)
                : "memory", "vcc", "scc", "v96", "v97", "v98", "v99", "v100", "v101", "v102", "v103", "v104", "v105", "v106", "v107", "v108", "v109", "v110", "v111", "v112", "v113", "v114", "v115", "v116", "v117", "v118", "v119", "v120", "v121", "v122", "v123", "v124", "v125", "v126", "v127", "v128", "v129", "v130", "v131", "v132", "v133", "v134", "v135", "v136", "v137", "v138", "v139", "v140", "v141", "v142", "v143", "v144", "v145", "v146", "v147", "v148", "v149", "v150", "v151", "v152", "v153", "v154", "v155", "v156", "v157", "v158", "v159", "v160", "v161", "v162", "v163", "v164", "v165", "v166", "v167");
        }
        STAMP(8);
        lwait(L + FRP + 256 * (ln & 3), 11);
        LAS char* pb = L + PART + (64 * kh + 32 * (w & 1) + (ln & 31)) * PPITCH + 16 * (ln >> 5);
#pragma unroll
        for (int g = 0; g < 4; ++g) *(LAS f32x4*)(pb + 32 * g) = (f32x4){o0[4 * g], o0[4 * g + 1], o0[4 * g + 2], o0[4 * g + 3]};
#pragma unroll
        for (int g = 0; g < 4; ++g) *(LAS f32x4*)(pb + 128 + 32 * g) = (f32x4){o1[4 * g], o1[4 * g + 1], o1[4 * g + 2], o1[4 * g + 3]};
        lset(L, L_PART + w, ln, 1);
        STAMP(9); STAMP_FLUSH();
        return;
    }

    const __amdgpu_buffer_rsrc_t ro = mkrs(p.out, S * D * 2);
    const v4u sx = mksrd(p.x, S * D * 2), sR = mksrd(R, W_BYTES);
    const int cw = w & 3;
    const int ftok = 64 * tt + 16 * cw + (w < 8 ? 8 : 0) + (lane >> 3), foff = ftok * 1024 + ct * 128 + (lane & 7) * 16;
    LAS char* ys = L + YST + cw * 2048;
    v4u xv; unsigned st;
    if (w < 8) {
        const v4u sd = mksrd(p.wd, F * D * 2);
        const unsigned sdo = (16 * 43 * (cw >> 1) + 8 * (cw & 1)) * 1024 + ct * 128;
        ald16(xv, sx, foff);
        lwait(FR(), 8);
#pragma unroll 1
        for (int i = 0; i < 23; ++i) dma16(sd, vd, sdo + WSTEP(i) * 16384, Lb + 4096 * i + 1024 * cw);
        WAIT_TIE(0, "+v"(xv)); ladd(L, L_WDF, lane);
        STAMP(3);
        __builtin_amdgcn_s_setprio(0);
        lwait(FL(L_CVD, 4), 1);
        __builtin_amdgcn_s_setprio(3);
#pragma unroll 1
        for (int i = 23; i < 37; ++i) dma16(sd, vd, sdo + WSTEP(i) * 16384, Lb + 4096 * i + 1024 * cw);
        WAIT_VM(0); lset(L, L_WDC + cw, lane, 1);
        STAMP(4);
        __builtin_amdgcn_s_setprio(0);
        lwait(FR(), 10);
        __builtin_amdgcn_s_setprio(3);
#pragma unroll 1
        for (int i = 37; i < 43; ++i) dma16(sd, vd, sdo + WSTEP(i) * 16384, Lb + 4096 * (i - 37) + 1024 * cw);
        ald4c(st, sR, W_STAT + ftok * 8);
        WAIT_VM(1); lset(L, L_WDC + cw, lane, 2);
        WAIT_TIE(0, "+v"(st));
        __builtin_amdgcn_s_setprio(0);
        STAMP(5);
    } else {
        const int ch = cw & 1, half = cw >> 1;
        __builtin_amdgcn_s_setprio(PRIO_C);
        {
            const v4u sk = mksrd(p.k, S * D * 2), sv = mksrd(p.v, S * D * 2), ss = mksrd(p.shk, D * 8);
            const unsigned vo = (64 * tt + 16 * cw + (lane >> 3)) * 1024 + ct * 128 + (lane & 7) * 16;
            dma16(sk, vo - 8192, 0, Lb + KIM + 2048 * cw);
            dma16(sk, vo, 0, Lb + KIM + 2048 * cw + 1024);
            dma16x2(sv, vo, vo + 8192 - 1024, 0, Lb + VIM + 2048 * cw);
            if (cw == 3) dma16(sk, vo + 8192, 0, Lb + KIM + 8192);
            if (cw == 2 && lane < 32) dma16(ss, lane * 16, ct * 512, Lb + SHI);
        }
        u64 z; unsigned d2_;
        asm volatile("s_load_dwordx2 %0, %2, 0x0\n\ts_load_dword %1, %3, 0x0\n\ts_waitcnt lgkmcnt(0)" : "=&s"(z), "=&s"(d2_) : "s"(p.ws), "s"(p.ssd + 4 * b) : "memory");
        const u64 tag = z + 1 + 2 * did;
        const unsigned tag32 = (unsigned)tag;
        const float dd = ch ? hi(d2_) : lo(d2_);
        if (cw == 0 && lane == 0) *(volatile LAS v2u*)(L + FLG + 4 * L_TAG) = (v2u){tag32, (unsigned)(tag >> 32)};
        WAIT_VM(0);
        lset(L, L_SV + cw, lane, 3);
        v4u xs[4];
        const int srow = 128 * pn + 8 * cg + 4 * ch + (lane >> 4);
        if (half == 0) {
            const v4u sw = mksrd(ch ? p.wu : p.wg, D * F * 2);
#pragma unroll
            for (int m = 0; m < 3; ++m) {
                const unsigned vo = (((lane + 64 * m) * 43691u) >> 19) * 2560 + lane * 16 + 1024 * m, so = 32 * pn * 2752 + w16 * 32, lo_ = Lb + (ch ? CSU : CSG) + m * 1024;
                dma16(sw, vo, so, lo_); dma16(sw, vo, so + 16 * 2752, lo_ + 3072);
            }
        } else {
#pragma unroll
            for (int i = 0; i < 4; ++i) ald16(xs[i], sx, srow * 1024 + ((lane & 15) + 16 * i) * 16);
        }
        STAMP(1);
        LAS char* kc = L + KC + ch * KCH;
        v4u fd[2]; v2u fe[2];
        {
            const int cc = lane & 15, tg = lane >> 4;
            lwait(FL(L_SV, 4), 3);
            STAMP(2);
            v2u kk[7], vv[4]; v4u sh[2];
#pragma unroll
            for (int i = 0; i < 7; ++i) kk[i] = *(LAS v2u*)(L + KIM + (16 * cw + 4 * tg + 5 + i) * 128 + cc * 8);
#pragma unroll
            for (int i = 0; i < 4; ++i) vv[i] = *(LAS v2u*)(L + VIM + (16 * cw + 4 * tg + i) * 128 + cc * 8);
#pragma unroll
            for (int i = 0; i < 2; ++i) sh[i] = *(LAS v4u*)(L + SHI + cc * 32 + 16 * i);
            LAS char* tw = L + (cc < 4 ? CS : STG) + 4 * cc * 128 + (((2 * cw + (tg >> 1)) ^ (cc & 7)) << 4) + ((tg & 1) ? 0 : 8);
#pragma unroll
            for (int i = 0; i < 4; ++i) {
                const unsigned s01 = sh[i >> 1][2 * (i & 1)], s23 = sh[i >> 1][2 * (i & 1) + 1];
                const float s0 = lo(s01), s1 = hi(s01), s2 = lo(s23), s3 = hi(s23);
                float kf[7], r[4];
#pragma unroll
                for (int t = 0; t < 7; ++t) kf[t] = (i & 1) ? hi(kk[t][i >> 1]) : lo(kk[t][i >> 1]);
#pragma unroll
                for (int t = 0; t < 4; ++t) {
                    const float a = __builtin_fmaf(kf[t + 3], s0, __builtin_fmaf(kf[t + 2], s1, __builtin_fmaf(kf[t + 1], s2, kf[t] * s3)));
                    r[t] = a * ((i & 1) ? hi(vv[t][i >> 1]) : lo(vv[t][i >> 1]));
                    asm("" : "+v"(r[t]));
                }
                *(LAS v2u*)(tw + i * 128) = (v2u){pk(r[3], r[2]), pk(r[1], r[0])};
            }
            lset(L, L_SV + cw, lane, 4);
            lwait(FL(L_SV, 4), 4);
#pragma unroll
            for (int i = 0; i < 2; ++i) {
                const int row = 16 * cw + 8 * i + (lane >> 3);
                const v4u t = *(LAS v4u*)(L + (cw ? STG : CS) + row * 128 + (lane & 7) * 16);
                st16c(t, rw, W_KVT + (64 * ct + row) * 4096 + tt * 128 + (((lane & 7) ^ ((row >> 2) & 7)) << 4));
            }
        }
        STAMP(3);
        if (half == 0) {
            const int hh = lane >> 5, blk = (lane >> 4) & 1, q = (lane & 15) >> 2, pp = lane & 3;
            LAS char* a = L + (blk ? CSU : CSG) + (8 * hh + q) * 192 + 96 * ch + 8 * pp;
            const bf16x8 ones = {0x3f80, 0x3f80, 0x3f80, 0x3f80, 0x3f80, 0x3f80, 0x3f80, 0x3f80};
            WAIT_VM(2);
            lset(L, L_CF + ch, lane, 1);
            lwait(FL(L_CF, 2), 1);
            unsigned cv[3];
#pragma unroll
            for (int u = 0; u < 3; ++u) {
                f32x16 c;
#pragma unroll
                for (int i = 0; i < 16; ++i) c[i] = 0.f;
                c = mfma32(ones, trd(a + 32 * u, 768), c); c = mfma32(ones, trd(a + 3072 + 32 * u, 768), c);
                const float c0 = c[0];
                cv[u] = __builtin_bit_cast(unsigned, c0);
            }
            if (lane < 32) st16c((v4u){tag32, cv[0], cv[1], cv[2]}, rw, W_CSP + (cg * 16 + pn) * 1024 + ch * 512 + lane * 16);
        } else {
            WAIT_TIE(2, "+v"(xs[0]), "+v"(xs[1]), "+v"(xs[2]), "+v"(xs[3]));
            float s = 0.f;
#pragma unroll
            for (int i = 0; i < 4; ++i)
#pragma unroll
                for (int e = 0; e < 4; ++e) s += lo(xs[i][e]) + hi(xs[i][e]);
            const float mean = sum16(s) * (1.f / D);
            float qq = 0.f;
#pragma unroll
            for (int i = 0; i < 4; ++i)
#pragma unroll
                for (int e = 0; e < 4; ++e) { const float a = lo(xs[i][e]) - mean, c = hi(xs[i][e]) - mean; qq = __builtin_fmaf(a, a, __builtin_fmaf(c, c, qq)); }
            const float var = rbf(sum16(qq) * (1.f / D));
            const unsigned stv = pk(mean, __builtin_sqrtf(rbf(var + 1e-5f)));
            if ((lane & 15) == 0) st8c(stv, tag32, rw, W_STAT + srow * 8);
        }
        lset(L, L_SV + cw, lane, 5);
        WAIT_VM(1);
        if (arrive(L, L_F0CNT, 4, lane) && lane < 32) gflag(R + W_IN0 + ((32 * ct + lane) * 32 + tt) * 8, tag);
        STAMP(4);
        lwait(FL(L_SV, 4), 5);
        dma16x2(mksrd(p.ssk, S * D * 2), lane * 16, lane * 16, (2 * b + ch) * 4096 + 2048 * half, Lb + KC + ch * KCH + 32 + 2048 * half);
        lwait<4>(FL(L_FULL + SWEEP_AT, 1), 4);
        if (cw == 0) {
            float c[3];
#pragma unroll 1
            for (unsigned s = 0; s < GSPIN; ++s) {
                v4u g[16];
#pragma unroll
                for (int i = 0; i < 16; ++i) g[i] = ld16c(rw, W_CSP + (cg * 16 + i) * 1024 + lane * 16);
                unsigned bad = 0;
                c[0] = 0.f; c[1] = 0.f; c[2] = 0.f;
#pragma unroll
                for (int i = 0; i < 16; ++i) { bad |= g[i].x ^ tag32; c[0] += asf(g[i].y); c[1] += asf(g[i].z); c[2] += asf(g[i].w); }
                if (__all(bad == 0)) break;
                __builtin_amdgcn_s_sleep(8);
            }
#pragma unroll
            for (int u = 0; u < 3; ++u) {
                const float h0 = rbf(c[u]), r0 = c[u] - h0, m0 = rbf(r0);
                *(LAS v2u*)(L + CS + (3 * (lane >> 5) + u) * 256 + (lane & 31) * 8) = (v2u){pk(h0, m0), pk(r0 - m0, 0.f)};
            }
            lset(L, L_PRE, lane, 1);
        } else if (cw == 1) {
            v4u t;
#pragma unroll 1
            for (unsigned s = 0; s < GSPIN; ++s) {
                t = ld16c(rw, W_STAT + 128 * pn * 8 + lane * 16);
                if (__all(t.y == tag32 && t.w == tag32)) break;
                __builtin_amdgcn_s_sleep(8);
            }
            *(LAS v2u*)(L + ST + lane * 8) = (v2u){t.x, t.z};
            lset(L, L_PRE + 1, lane, 1);
        }
        if (cw == 3) { gwait(R + W_IN0 + (32 * b + (lane & 31)) * 8, tag); lset(L, L_CGO, lane, 1); }
        WAIT_VM(0);
        lset(L, L_SV + cw, lane, 6);
        STAMP(5);
        lwait(FL(L_CGO, 1), 1);
        STAMP(6);
        LAS char* ub = L + UB + ch * UCH + 512;
        {
            v4u uu[2];
#pragma unroll
            for (int i = 0; i < 2; ++i) ald16c(uu[i], sR, W_KVT + (2 * b + ch) * 4096 + (128 * half + lane + 64 * i) * 16);
            ald16(xv, sx, foff);
            lwait(FL(L_SV + (cw ^ 2), 1), 6);
            {
#pragma unroll
                for (int i = 0; i < 2; ++i) { LAS char* o = kc + 32 + (128 * half + lane + 64 * i) * 16; fd[i] = *(LAS v4u*)o; fe[i] = *(LAS v2u*)(o + 16); }
                const unsigned f0 = rfl(fd[0].x), f1 = rfl(fd[0].y);
#pragma unroll
                for (int i = 0; i < 3; ++i) {
                    v4u d; v2u e; int m;
                    if (i < 2) { d = fd[i]; e = fe[i]; m = 128 * half + lane + 64 * i; }
                    else { d = (v4u){0u, 0u, 0u, 0u}; e = lane ? (v2u){f0, f1} : (v2u){0u, 0u}; m = lane - 2; }
                    const unsigned a0 = __builtin_amdgcn_alignbyte(d.y, d.x, 2), a1 = __builtin_amdgcn_alignbyte(d.z, d.y, 2), a2 = __builtin_amdgcn_alignbyte(d.w, d.z, 2),
                                   a3 = __builtin_amdgcn_alignbyte(e.x, d.w, 2), a4 = __builtin_amdgcn_alignbyte(e.y, e.x, 2);
                    if (i < 2 || (lane < 2 && half == 0)) {
                        LAS char* o = kc + 32 + 16 * m;
                        if (i == 2) *(LAS v4u*)o = d;
                        *(LAS v4u*)(o + KCP) = (v4u){a0, a1, a2, a3};
                        *(LAS v4u*)(o + 2 * KCP) = (v4u){d.y, d.z, d.w, e.x}; *(LAS v4u*)(o + 3 * KCP) = (v4u){a1, a2, a3, a4};
                    }
                }
            }
            lset(L, L_CV1 + cw, lane, 1);
            lwait(FR(), 8);
            WAIT_TIE(1, "+v"(uu[0]), "+v"(uu[1]));
            STAMP(14);
            if (lane < 32 && half == 0) *(LAS v4u*)(ub - 512 + lane * 16) = (v4u){0u, 0u, 0u, 0u};
#pragma unroll
            for (int i = 0; i < 2; ++i) *(LAS v4u*)(ub + (128 * half + lane + 64 * i) * 16) = uu[i];
            lset(L, L_CV1 + cw, lane, 2);
        }
        lwait(FL(L_CV1 + (cw ^ 2), 1), 1);
        __builtin_amdgcn_s_setprio(1);
        lwait(FR(), 9);
        STAMP(7);
        const int a16 = lane & 15, g = lane >> 4;
        f32x4 acc[8];
#pragma unroll
        for (int n = 0; n < 8; ++n) acc[n] = (f32x4){0.f, 0.f, 0.f, 0.f};
        {
            const int o0 = 128 * half + 16 * (g >> 1) - 8 * (g & 1) + a16 - 7, sft = o0 & 3;
            unsigned pa = Lb + KC + ch * KCH + sft * KCP + 32 + 2 * (o0 - sft);
            unsigned pb = Lb + UB + ch * UCH + 512 + 2 * (16 * (a16 - 8 * half - (g >> 1)) + 8 * (g & 1)) - 64, it = 0, vf, spin;
            asm volatile(
                "ds_read_b64 v[104:105], %8 offset:0\n\t"
                "ds_read_b64 v[106:107], %8 offset:8\n\t"
                "ds_read_b64 v[108:109], %8 offset:512\n\t"
                "ds_read_b64 v[110:111], %8 offset:520\n\t"
                "ds_read_b64 v[112:113], %8 offset:1024\n\t"
                "ds_read_b64 v[114:115], %8 offset:1032\n\t"
                "ds_read_b64 v[116:117], %8 offset:1536\n\t"
                "ds_read_b64 v[118:119], %8 offset:1544\n\t"
                "ds_read_b64 v[120:121], %8 offset:2048\n\t"
                "ds_read_b64 v[122:123], %8 offset:2056\n\t"
                "ds_read_b64 v[124:125], %8 offset:2560\n\t"
                "ds_read_b64 v[126:127], %8 offset:2568\n\t"
                "ds_read_b64 v[128:129], %8 offset:3072\n\t"
                "ds_read_b64 v[130:131], %8 offset:3080\n\t"
                "ds_read_b64 v[132:133], %8 offset:3584\n\t"
                "ds_read_b64 v[134:135], %8 offset:3592\n\t"
                "s_branch Lcv_slow_%=\n\t"
                "Lcv_back_%=:\n\t"
                "ds_read_b128 v[164:167], %9 offset:3648\n\t"
                "ds_read_b128 v[160:163], %9 offset:3136\n\t"
                "ds_read_b128 v[156:159], %9 offset:2624\n\t"
                "ds_read_b128 v[152:155], %9 offset:2112\n\t"
                "ds_read_b128 v[148:151], %9 offset:1600\n\t"
                "ds_read_b128 v[144:147], %9 offset:1088\n\t"
                "ds_read_b128 v[140:143], %9 offset:576\n\t"
                "ds_read_b128 v[136:139], %9 offset:64\n\t"
                "s_waitcnt lgkmcnt(0)\n\t"
                "Lcv_loop_%=:\n\t"
                "v_mfma_f32_16x16x32_bf16 %7, v[104:107], v[164:167], %7\n\t"
                "v_mfma_f32_16x16x32_bf16 %6, v[104:107], v[160:163], %6\n\t"
                "v_mfma_f32_16x16x32_bf16 %5, v[104:107], v[156:159], %5\n\t"
                "v_mfma_f32_16x16x32_bf16 %4, v[104:107], v[152:155], %4\n\t"
                "s_waitcnt lgkmcnt(8)\n\t"
                "v_mfma_f32_16x16x32_bf16 %3, v[104:107], v[148:151], %3\n\t"
                "v_mfma_f32_16x16x32_bf16 %2, v[104:107], v[144:147], %2\n\t"
                "s_waitcnt lgkmcnt(5)\n\t"
                "v_mfma_f32_16x16x32_bf16 %1, v[104:107], v[140:143], %1\n\t"
                "s_waitcnt lgkmcnt(2)\n\t"
                "v_mfma_f32_16x16x32_bf16 %0, v[104:107], v[136:139], %0\n\t"
                "s_waitcnt lgkmcnt(0)\n\t"
                "v_mfma_f32_16x16x32_bf16 %7, v[108:111], v[160:163], %7\n\t"
                "ds_read_b128 v[164:167], %9 offset:3584\n\t"
                "v_mfma_f32_16x16x32_bf16 %6, v[108:111], v[156:159], %6\n\t"
                "ds_read_b64 v[104:105], %8 offset:64\n\t"
                "v_mfma_f32_16x16x32_bf16 %5, v[108:111], v[152:155], %5\n\t"
                "ds_read_b64 v[106:107], %8 offset:72\n\t"
                "v_mfma_f32_16x16x32_bf16 %4, v[108:111], v[148:151], %4\n\t"
                "v_mfma_f32_16x16x32_bf16 %3, v[108:111], v[144:147], %3\n\t"
                "v_mfma_f32_16x16x32_bf16 %2, v[108:111], v[140:143], %2\n\t"
                "v_mfma_f32_16x16x32_bf16 %1, v[108:111], v[136:139], %1\n\t"
                "v_mfma_f32_16x16x32_bf16 %7, v[112:115], v[156:159], %7\n\t"
                "ds_read_b128 v[160:163], %9 offset:3072\n\t"
                "v_mfma_f32_16x16x32_bf16 %6, v[112:115], v[152:155], %6\n\t"
                "ds_read_b64 v[108:109], %8 offset:576\n\t"
                "v_mfma_f32_16x16x32_bf16 %5, v[112:115], v[148:151], %5\n\t"
                "ds_read_b64 v[110:111], %8 offset:584\n\t"
                "v_mfma_f32_16x16x32_bf16 %4, v[112:115], v[144:147], %4\n\t"
                "v_mfma_f32_16x16x32_bf16 %3, v[112:115], v[140:143], %3\n\t"
                "v_mfma_f32_16x16x32_bf16 %2, v[112:115], v[136:139], %2\n\t"
                "v_mfma_f32_16x16x32_bf16 %7, v[116:119], v[152:155], %7\n\t"
                "ds_read_b128 v[156:159], %9 offset:2560\n\t"
                "v_mfma_f32_16x16x32_bf16 %6, v[116:119], v[148:151], %6\n\t"
                "ds_read_b64 v[112:113], %8 offset:1088\n\t"
                "v_mfma_f32_16x16x32_bf16 %5, v[116:119], v[144:147], %5\n\t"
                "ds_read_b64 v[114:115], %8 offset:1096\n\t"
                "v_mfma_f32_16x16x32_bf16 %4, v[116:119], v[140:143], %4\n\t"
                "v_mfma_f32_16x16x32_bf16 %3, v[116:119], v[136:139], %3\n\t"
                "v_mfma_f32_16x16x32_bf16 %7, v[120:123], v[148:151], %7\n\t"
                "ds_read_b128 v[152:155], %9 offset:2048\n\t"
                "v_mfma_f32_16x16x32_bf16 %6, v[120:123], v[144:147], %6\n\t"
                "ds_read_b64 v[116:117], %8 offset:1600\n\t"
                "v_mfma_f32_16x16x32_bf16 %5, v[120:123], v[140:143], %5\n\t"
                "ds_read_b64 v[118:119], %8 offset:1608\n\t"
                "v_mfma_f32_16x16x32_bf16 %4, v[120:123], v[136:139], %4\n\t"
                "v_mfma_f32_16x16x32_bf16 %7, v[124:127], v[144:147], %7\n\t"
                "ds_read_b128 v[148:151], %9 offset:1536\n\t"
                "v_mfma_f32_16x16x32_bf16 %6, v[124:127], v[140:143], %6\n\t"
                "ds_read_b64 v[120:121], %8 offset:2112\n\t"
                "v_mfma_f32_16x16x32_bf16 %5, v[124:127], v[136:139], %5\n\t"
                "ds_read_b64 v[122:123], %8 offset:2120\n\t"
                "v_mfma_f32_16x16x32_bf16 %7, v[128:131], v[140:143], %7\n\t"
                "ds_read_b128 v[144:147], %9 offset:1024\n\t"
                "v_mfma_f32_16x16x32_bf16 %6, v[128:131], v[136:139], %6\n\t"
                "ds_read_b64 v[124:125], %8 offset:2624\n\t"
                "v_mfma_f32_16x16x32_bf16 %7, v[132:135], v[136:139], %7\n\t"
                "ds_read_b64 v[126:127], %8 offset:2632\n\t"
                "ds_read_b128 v[140:143], %9 offset:512\n\t"
                "ds_read_b64 v[128:129], %8 offset:3136\n\t"
                "ds_read_b64 v[130:131], %8 offset:3144\n\t"
                "ds_read_b128 v[136:139], %9 offset:0\n\t"
                "ds_read_b64 v[132:133], %8 offset:3648\n\t"
                "ds_read_b64 v[134:135], %8 offset:3656\n\t"
                "v_add_u32 %8, 64, %8\n\t"
                "v_subrev_u32 %9, 64, %9\n\t"
                "s_add_u32 %10, %10, 1\n\t"
                "s_cmp_lt_u32 %10, 4\n\t"
                "s_waitcnt lgkmcnt(11)\n\t"
                "s_cbranch_scc1 Lcv_loop_%=\n\t"
                "s_branch Lcv_done_%=\n\t"
                "Lcv_slow_%=:\n\t"
                "s_mov_b32 %12, 0x400000\n\t"
                "Lcv_slows_%=:\n\t"
                "ds_read_b32 %11, %13\n\t"
                "s_sub_u32 %12, %12, 1\n\t"
                "s_cbranch_scc1 Lcv_back_%=\n\t"
                "s_waitcnt lgkmcnt(0)\n\t"
                "v_cmp_gt_u32 vcc, 2, %11\n\t"
                "s_cbranch_vccnz Lcv_slows_%=\n\t"
                "s_branch Lcv_back_%=\n\t"
                "Lcv_done_%=:\n\t"
                "s_waitcnt lgkmcnt(0)\n\t"
                "s_nop 7\n\t"
                : "+v"(acc[0]), "+v"(acc[1]), "+v"(acc[2]), "+v"(acc[3]), "+v"(acc[4]), "+v"(acc[5]), "+v"(acc[6]), "+v"(acc[7]), "+v"(pa), "+v"(pb), "+s"(it),
                  "=&v"(vf), "=&s"(spin)
                : "v"(Lb + FLG + 4 * (L_CV1 + (cw ^ 2)))
                : "memory", "vcc", "scc", "v104", "v105", "v106", "v107", "v108", "v109", "v110", "v111", "v112", "v113", "v114", "v115", "v116", "v117", "v118", "v119", "v120", "v121", "v122", "v123", "v124", "v125", "v126", "v127", "v128", "v129", "v130", "v131", "v132", "v133", "v134", "v135", "v136", "v137", "v138", "v139", "v140", "v141", "v142", "v143", "v144", "v145", "v146", "v147", "v148", "v149", "v150", "v151", "v152", "v153", "v154", "v155", "v156", "v157", "v158", "v159", "v160", "v161", "v162", "v163", "v164", "v165", "v166", "v167");
        }
        STAMP(8);
        __builtin_amdgcn_s_setprio(3);
        LAS f32x4* red = (LAS f32x4*)(L + RED);
        if (half) {
#pragma unroll
            for (int n = 0; n < 4; ++n) { red[(cw * 4 + n) * 64 + lane] = acc[n]; acc[n] = acc[4 + n]; }
        } else {
#pragma unroll
            for (int n = 0; n < 4; ++n) red[(cw * 4 + n) * 64 + lane] = acc[4 + n];
        }
        lset(L, L_CV2 + cw, lane, 1);
        v2u kv[4]; f32x4 t[4];
#pragma unroll
        for (int n = 0; n < 4; ++n) kv[n] = *(LAS v2u*)(ub + 2 * (256 * (4 * half + n) + 16 * a16 + 8 * (g >> 1) + ((g & 1) ? 0 : 4)));
#pragma unroll 1
        for (unsigned s = 0; s < LSPIN; ++s) {
            const unsigned f = lrd(FL(L_CV2 + (cw ^ 2), 1));
#pragma unroll
            for (int n = 0; n < 4; ++n) t[n] = *(volatile LAS f32x4*)(red + ((cw ^ 2) * 4 + n) * 64 + lane);
            if (__all(f >= 1)) break;
        }
        lset(L, L_CVD + cw, lane, 1);
        v2u yv[4];
#pragma unroll
        for (int n = 0; n < 4; ++n) {
            t[n] += acc[n];
            yv[n] = (v2u){pk(__builtin_fmaf(hi(kv[n].y), dd, t[n][0]), __builtin_fmaf(lo(kv[n].y), dd, t[n][1])),
                          pk(__builtin_fmaf(hi(kv[n].x), dd, t[n][2]), __builtin_fmaf(lo(kv[n].x), dd, t[n][3]))};
        }
#pragma unroll
        for (int n = 0; n < 4; n += 2) {
            const v2u s0 = __builtin_amdgcn_permlane16_swap(yv[n].x, yv[n + 1].x, false, false), s1 = __builtin_amdgcn_permlane16_swap(yv[n].y, yv[n + 1].y, false, false);
            st16c((v4u){s0.x, s1.x, s0.y, s1.y}, rw, W_YT + (2 * b + ch) * 4096 + 2 * (256 * (4 * half + n + (g & 1)) + 16 * a16 + 8 * (g >> 1)));
        }
        WAIT_TIE(0, "+v"(xv));
        if (arrive(L, L_FYCNT, 4, lane) && lane < 32) gflag(R + W_INY + ((lane * 8 + (b >> 5)) * 32 + (b & 31)) * 8, tag);
        STAMP(9);
        if (cw == 3) { gwait(R + W_INY + ((tt * 8 + ct) * 32 + (lane & 31)) * 8, tag); lset(L, L_CGO, lane, 2); } else lwait(FL(L_CGO, 1), 2);
        STAMP(10);
        const int yo = W_YT + (64 * ct + lane) * 4096 + tt * 128 + cw * 32;
        v4u y0, y1;
        ald16c(y0, sR, yo); ald16c(y1, sR, yo + 16);
        lwait(FR(), 10);
        ald4c(st, sR, W_STAT + ftok * 8);
        if (b == 0 && cw == 0 && lane == 0) gflag(p.ws, tag);
        lwait(FL(L_CVD, 4), 1);
        WAIT_TIE(0, "+v"(y0), "+v"(y1), "+v"(st));
        *(LAS v4u*)(ys + lane * 32) = y0; *(LAS v4u*)(ys + lane * 32 + 16) = y1;
        MEMBAR();
        LAS char* a = ys + (8 * (lane >> 4) + ((lane & 15) >> 2)) * 32 + 8 * (lane & 3);
        const bf16x8 t0_ = trd(a, 128), t1_ = trd(a + 1024, 128);
        MEMBAR();
        *(LAS bf16x8*)(ys + (lane & 15) * 128 + (lane >> 4) * 16) = t0_; *(LAS bf16x8*)(ys + (lane & 15) * 128 + 64 + (lane >> 4) * 16) = t1_;
        lset(L, L_YT + cw, lane, 1);
    }
    float xn[8];
    {
        const float mb = lo(st), rs = __builtin_amdgcn_rcpf(hi(st));
#pragma unroll
        for (int e = 0; e < 4; ++e) { xn[2 * e] = (lo(xv[e]) - mb) * rs; xn[2 * e + 1] = (hi(xv[e]) - mb) * rs; }
#pragma unroll
        for (int e = 0; e < 8; ++e) asm volatile("" : "+v"(xn[e]));
        lwait(FL(L_YT + cw, 1), 1);
        const v4u y = *(LAS v4u*)(ys + (w < 8 ? 1024 : 0) + lane * 16);
#pragma unroll
        for (int e = 0; e < 4; ++e) { xn[2 * e] *= lo(y[e]); xn[2 * e + 1] *= hi(y[e]); }
#pragma unroll
        for (int e = 0; e < 8; ++e) asm volatile("" : "+v"(xn[e]));
    }
    STAMP(11);
    __builtin_amdgcn_s_setprio(0);
    lwait(FL(L_PART, 4), 1);
    LAS char* pr = L + PART + (ftok & 63) * PPITCH + (lane & 7) * 32;
    const f32x4 pa_[4] = {*(LAS f32x4*)pr, *(LAS f32x4*)(pr + 16), *(LAS f32x4*)(pr + 64 * PPITCH), *(LAS f32x4*)(pr + 64 * PPITCH + 16)};
    STAMP(12);
    {
        const f32x4 a0 = pa_[0] + pa_[2], a1 = pa_[1] + pa_[3];
        float r[8];
#pragma unroll
        for (int e = 0; e < 8; ++e) {
            const float o = e < 4 ? a0[e & 3] : a1[e & 3];
            const float gt = o * __builtin_amdgcn_rcpf(1.f + __builtin_amdgcn_exp2f(-1.442695041f * o));
            r[e] = __builtin_fmaf(xn[e], gt, (e & 1) ? hi(xv[e >> 1]) : lo(xv[e >> 1]));
        }
        __builtin_amdgcn_raw_buffer_store_b128((v4u){pk(r[0], r[1]), pk(r[2], r[3]), pk(r[4], r[5]), pk(r[6], r[7])}, ro, foff, 0, OUT_AUX);
    }
    STAMP(13);
    STAMP_FLUSH();
}

extern "C" void kernel_launch(void* const* d_in, const int* in_sizes, int n_in, void* d_out, int out_size, void* d_ws, size_t ws_size,
                              hipStream_t stream) {
    Params p;
    p.x = (const char*)d_in[0]; p.k = (const char*)d_in[1]; p.v = (const char*)d_in[2]; p.shk = (const char*)d_in[3];
    p.ssk = (const char*)d_in[4]; p.ssd = (const char*)d_in[5]; p.wg = (const char*)d_in[7]; p.wu = (const char*)d_in[8];
    p.wd = (const char*)d_in[9]; p.out = (char*)d_out; p.ws = (char*)d_ws;
    (void)hipFuncSetAttribute((const void*)fused_h3_geglu, hipFuncAttributeMaxDynamicSharedMemorySize, LDS_BYTES);
    fused_h3_geglu<<<256, 768, LDS_BYTES, stream>>>(p);
}
```
